```python
import torch

def _softmax(x, axis=-1):
    return torch.softmax(x, dim=axis)
def _gelu(x):
    return torch.nn.functional.gelu(x, approximate="tanh")
def _silu(x):
    return torch.nn.functional.silu(x)
def _sigmoid(x):
    return torch.sigmoid(x)
def _layernorm(x, axis=-1, eps=1e-5):
    m = x.mean(dim=axis, keepdim=True)
    v = x.var(dim=axis, keepdim=True, unbiased=False)
    return (x - m) / torch.sqrt(v + eps)
def _scatter_add(tgt, idx, src, axis=0):
    return tgt.clone().index_add_(axis, idx, src)
def _segment_sum(src, seg, n):
    out = torch.zeros((n,) + src.shape[1:], dtype=src.dtype)
    return out.index_add_(0, seg, src)
def _pad(x, pw):
    flat = tuple(v for pair in reversed(pw) for v in pair)
    return torch.nn.functional.pad(x, flat)

kernel_name = 'recurrent_scan__ssm_conv_s5__glu_gated_glu__prompt'

def setup_inputs():

    _w = (lambda a: (lambda t: t.to(torch.bfloat16) if t.is_floating_point() else t)(torch.as_tensor(a)))
    def _A_setup():
        B, S, D = 1, 2048, 512
        P = D
        Lambda_re = -0.5 * (lambda _x: torch.exp(torch.as_tensor(_x)))((lambda *s: torch.randn(*s, dtype=torch.bfloat16) * 0.2)(P))
        Lambda_im = (lambda *s: torch.randn(*s, dtype=torch.bfloat16) * 0.4)(P) * 1.0
        B_re = (lambda *s: torch.randn(*s, dtype=torch.bfloat16) * 0.2)(P, D) * (D ** -0.5)
        B_im = (lambda *s: torch.randn(*s, dtype=torch.bfloat16) * 0.2)(P, D) * (D ** -0.5)
        C_re = (lambda *s: torch.randn(*s, dtype=torch.bfloat16) * 0.2)(D, P) * (P ** -0.5)
        C_im = (lambda *s: torch.randn(*s, dtype=torch.bfloat16) * 0.2)(D, P) * (P ** -0.5)
        log_dt = (lambda *s: torch.randn(*s, dtype=torch.bfloat16) * 0.2)(P) * 0.3 - 2.0
        Dskip = (lambda *s: torch.randn(*s, dtype=torch.bfloat16) * 0.05)(D)
        return {
            "x": _w((lambda *s: torch.randn(*s, dtype=torch.bfloat16) * 0.05)(B, S, D)),
            "Lambda_re": _w(Lambda_re),
            "Lambda_im": _w(Lambda_im),
            "B_re": _w(B_re),
            "B_im": _w(B_im),
            "C_re": _w(C_re),
            "C_im": _w(C_im),
            "log_dt": _w(log_dt),
            "Dskip": _w(Dskip),
        }
    _A_in = _A_setup()

    def _B_setup():
        B, S, D, F = 1, 2048, 512, 1792
        return {
            "x": _w((lambda *s: torch.randn(*s, dtype=torch.bfloat16) * 0.05)(B, S, D)),
            "w_gate": _w((lambda *s: torch.randn(*s, dtype=torch.bfloat16) * 0.05)(D, F)),
            "w_up": _w((lambda *s: torch.randn(*s, dtype=torch.bfloat16) * 0.05)(D, F)),
            "w_down": _w((lambda *s: torch.randn(*s, dtype=torch.bfloat16) * 0.05)(F, D)),
        }
    _B_in = _B_setup()

    return {
        "A_x": _A_in["x"],
        "A_Lambda_re": _A_in["Lambda_re"],
        "A_Lambda_im": _A_in["Lambda_im"],
        "A_B_re": _A_in["B_re"],
        "A_B_im": _A_in["B_im"],
        "A_C_re": _A_in["C_re"],
        "A_C_im": _A_in["C_im"],
        "A_log_dt": _A_in["log_dt"],
        "A_Dskip": _A_in["Dskip"],
        "B_x": _B_in["x"],
        "B_w_gate": _B_in["w_gate"],
        "B_w_up": _B_in["w_up"],
        "B_w_down": _B_in["w_down"],
    }

def reference(A_x, A_Lambda_re, A_Lambda_im, A_B_re, A_B_im, A_C_re, A_C_im, A_log_dt, A_Dskip, B_x, B_w_gate, B_w_up, B_w_down):
    def _A_ref(x, Lambda_re, Lambda_im, B_re, B_im, C_re, C_im, log_dt, Dskip):
        Bsz, L, H = x.shape
        Lam = Lambda_re + 1j * Lambda_im
        Bmat = B_re + 1j * B_im
        Cmat = C_re + 1j * C_im
        dt = torch.exp(log_dt)
        Lam_bar = torch.exp(dt * Lam)
        B_bar = ((Lam_bar - 1.0) / Lam)[:, None] * Bmat
        Bu = torch.einsum("bld,pd->blp", x + 0j, B_bar)
        h = torch.zeros_like(Bu[:, 0, :])
        hs = []
        for t in range(L):
            h = Lam_bar[None, :] * h + Bu[:, t, :]
            hs.append(h)
        hseq = torch.stack(hs, 1)
        y = 2.0 * torch.einsum("blp,dp->bld", hseq, Cmat).real + x * Dskip[None, None, :]
        return y

    def _B_ref(x, w_gate, w_up, w_down):
        g = torch.einsum("bsd,df->bsf", x, w_gate)
        u = torch.einsum("bsd,df->bsf", x, w_up)
        h = _sigmoid(g) * u
        return torch.einsum("bsf,fd->bsd", h, w_down)

    x = A_x
    xn = _layernorm(x, axis=-1)
    s = _A_ref(xn, A_Lambda_re, A_Lambda_im, A_B_re, A_B_im, A_C_re, A_C_im, A_log_dt, A_Dskip)
    g = _silu(_B_ref(xn, B_w_gate, B_w_up, B_w_down))
    return x + s * g
```

```cpp
#include <hip/hip_runtime.h>
#include <hip/hip_bf16.h>
typedef __hip_bfloat16 bf16;
__device__ __forceinline__ float bf2f(bf16 v){return __bfloat162float(v);}
__device__ __forceinline__ float rbf(float v){return __bfloat162float(__float2bfloat16(v));}
constexpr int S=2048,D=512,P=512,F=1792;

__global__ void ln_k(const bf16* x, bf16* xn){
  int r=blockIdx.x; __shared__ float red[256];
  float a=bf2f(x[r*D+threadIdx.x]), b=bf2f(x[r*D+256+threadIdx.x]);
  red[threadIdx.x]=a+b; __syncthreads();
  for(int s=128;s>0;s>>=1){ if(threadIdx.x<s) red[threadIdx.x]+=red[threadIdx.x+s]; __syncthreads();}
  float mean=red[0]/D; __syncthreads();
  red[threadIdx.x]=(a-mean)*(a-mean)+(b-mean)*(b-mean); __syncthreads();
  for(int s=128;s>0;s>>=1){ if(threadIdx.x<s) red[threadIdx.x]+=red[threadIdx.x+s]; __syncthreads();}
  float var=red[0]/D;
  float m=rbf(mean), v=rbf(var), sd=rbf(sqrtf(rbf(v+1e-5f)));
  xn[r*D+threadIdx.x]=__float2bfloat16(rbf(a-m)/sd);
  xn[r*D+256+threadIdx.x]=__float2bfloat16(rbf(b-m)/sd);
}
__global__ void bu_k(const bf16* xn,const bf16* Bre,const bf16* Bim,float2* v){
  int t=blockIdx.x; __shared__ float xs[D];
  for(int i=threadIdx.x;i<D;i+=blockDim.x) xs[i]=bf2f(xn[t*D+i]); __syncthreads();
  for(int p=threadIdx.x;p<P;p+=blockDim.x){ float re=0,im=0;
    for(int d=0;d<D;d++){re+=xs[d]*bf2f(Bre[p*D+d]); im+=xs[d]*bf2f(Bim[p*D+d]);}
    v[t*P+p]=make_float2(re,im);} }
__global__ void scan_k(const bf16* Lre,const bf16* Lim,const bf16* ldt,float2* v){
  int p=blockIdx.x*blockDim.x+threadIdx.x; if(p>=P) return;
  float lr=bf2f(Lre[p]), li=bf2f(Lim[p]), dt=rbf(expf(bf2f(ldt[p])));
  float er=expf(dt*lr), sn,cs; sincosf(dt*li,&sn,&cs); float ar=er*cs, ai=er*sn;
  float nr=ar-1.f, ni=ai, den=lr*lr+li*li; float cr=(nr*lr+ni*li)/den, ci=(ni*lr-nr*li)/den;
  float hr=0,hi=0;
  for(int t=0;t<S;t++){ float2 u=v[t*P+p]; float br=cr*u.x-ci*u.y, bi=cr*u.y+ci*u.x;
    float nhr=ar*hr-ai*hi+br, nhi=ar*hi+ai*hr+bi; hr=nhr;hi=nhi; v[t*P+p]=make_float2(hr,hi);} }
__global__ void glu_k(const bf16* xn,const bf16* wg,const bf16* wu,bf16* hm){
  int t=blockIdx.x; __shared__ float xs[D];
  for(int i=threadIdx.x;i<D;i+=blockDim.x) xs[i]=bf2f(xn[t*D+i]); __syncthreads();
  for(int f=threadIdx.x;f<F;f+=blockDim.x){ float g=0,u=0;
    for(int d=0;d<D;d++){g+=xs[d]*bf2f(wg[d*F+f]); u+=xs[d]*bf2f(wu[d*F+f]);}
    g=rbf(g);u=rbf(u); float s=rbf(1.f/(1.f+expf(-g))); hm[t*F+f]=__float2bfloat16(s*u);} }
__global__ void fin_k(const bf16* x,const bf16* xn,const float2* h,const bf16* Cre,const bf16* Cim,const bf16* Dk,const bf16* hm,const bf16* wd,float* out){
  int t=blockIdx.x; __shared__ float2 hs[P]; __shared__ float ms[F];
  for(int i=threadIdx.x;i<P;i+=blockDim.x) hs[i]=h[t*P+i];
  for(int i=threadIdx.x;i<F;i+=blockDim.x) ms[i]=bf2f(hm[t*F+i]); __syncthreads();
  for(int d=threadIdx.x;d<D;d+=blockDim.x){ float y=0,g=0;
    for(int p=0;p<P;p++) y+=hs[p].x*bf2f(Cre[d*P+p])-hs[p].y*bf2f(Cim[d*P+p]);
    for(int f=0;f<F;f++) g+=ms[f]*bf2f(wd[f*D+d]);
    y=2.f*y+rbf(bf2f(xn[t*D+d])*bf2f(Dk[d])); g=rbf(g); g=rbf(g/(1.f+expf(-g)));
    out[t*D+d]=bf2f(x[t*D+d])+y*g; } }
extern "C" void kernel_launch(void* const* d_in, const int* in_sizes, int n_in, void* d_out, int out_size, void* d_ws, size_t ws_size, hipStream_t stream){
  const bf16* x=(const bf16*)d_in[0]; char* ws=(char*)d_ws;
  bf16* xn=(bf16*)ws; float2* v=(float2*)(ws+(4<<20)); bf16* hm=(bf16*)(ws+(16<<20));
  ln_k<<<S,256,0,stream>>>(x,xn);
  bu_k<<<S,256,0,stream>>>(xn,(const bf16*)d_in[3],(const bf16*)d_in[4],v);
  scan_k<<<8,64,0,stream>>>((const bf16*)d_in[1],(const bf16*)d_in[2],(const bf16*)d_in[7],v);
  glu_k<<<S,256,0,stream>>>(xn,(const bf16*)d_in[10],(const bf16*)d_in[11],hm);
  fin_k<<<S,256,0,stream>>>(x,xn,v,(const bf16*)d_in[5],(const bf16*)d_in[6],(const bf16*)d_in[8],hm,(const bf16*)d_in[12],(float*)d_out);
}
```

```cpp
#include <hip/hip_runtime.h>
#include <hip/hip_bf16.h>
#include <stdint.h>

typedef unsigned short u16;
typedef __attribute__((ext_vector_type(8))) short bf16x8;
typedef __attribute__((ext_vector_type(4))) short s16x4;
typedef __attribute__((ext_vector_type(4))) float f32x4;
typedef __attribute__((ext_vector_type(2))) float f32x2;
typedef __attribute__((ext_vector_type(4))) unsigned u32x4;
typedef __attribute__((ext_vector_type(2))) unsigned u32x2;
typedef __attribute__((ext_vector_type(2))) __bf16 bf16x2_t;
#define LAS __attribute__((address_space(3)))

constexpr int S_ = 2048, D_ = 512, F_ = 1792;
constexpr int KS_TOT = 88;

constexpr size_t OFF_A   = 0;
constexpr size_t OFF_B   = OFF_A + (size_t)32 * 88 * 4096;
constexpr size_t OFF_V   = OFF_B + (size_t)8 * 88 * 4096;
constexpr size_t OFF_AGG = OFF_V + (size_t)128 * 64 * 1024;
constexpr size_t OFF_ST  = OFF_AGG + (size_t)128 * 64 * 64;
constexpr size_t OFF_CT  = OFF_ST + (size_t)2048 * 8;
constexpr size_t OFF_FL  = OFF_CT + (size_t)256 * 128;

constexpr int L1_PW   = 147456;
constexpr int L1_LAM  = L1_PW + 9216;
constexpr int L1_CS   = L1_LAM + 576;
constexpr int L1_CS8  = L1_CS + 512;
constexpr int L1_SIZE = L1_CS8 + 512;

__device__ __forceinline__ float bf2f(unsigned h) { return __builtin_bit_cast(float, h << 16); }
__device__ __forceinline__ float rbf(float v) { return (float)(__bf16)v; }
__device__ __forceinline__ unsigned pk2(float lo, float hi) {
  bf16x2_t r = {(__bf16)lo, (__bf16)hi};
  return __builtin_bit_cast(unsigned, r);
}
__device__ __forceinline__ float dot2(unsigned a, unsigned b, float c) {
  return __builtin_amdgcn_fdot2_f32_bf16(__builtin_bit_cast(bf16x2_t, a), __builtin_bit_cast(bf16x2_t, b), c, false);
}
template <int CTRL> __device__ __forceinline__ float dpp0(float x) {
  return __builtin_bit_cast(float, __builtin_amdgcn_update_dpp(0, __builtin_bit_cast(int, x), CTRL, 0xf, 0xf, true));
}
__device__ __forceinline__ f32x4 cmul(f32x4 a, f32x4 b) {
  f32x4 r;
  r[0] = a[0] * b[0] - a[2] * b[2];
  r[1] = a[1] * b[1] - a[3] * b[3];
  r[2] = a[0] * b[2] + a[2] * b[0];
  r[3] = a[1] * b[3] + a[3] * b[1];
  return r;
}
__device__ __forceinline__ f32x4 cfma(f32x4 a, f32x4 b, f32x4 c) {
  f32x4 r;
  r[0] = fmaf(a[0], b[0], fmaf(-a[2], b[2], c[0]));
  r[1] = fmaf(a[1], b[1], fmaf(-a[3], b[3], c[1]));
  r[2] = fmaf(a[0], b[2], fmaf(a[2], b[0], c[2]));
  r[3] = fmaf(a[1], b[3], fmaf(a[3], b[1], c[3]));
  return r;
}
#define MFMA(a, b, c) __builtin_amdgcn_mfma_f32_16x16x32_bf16(a, b, c, 0, 0, 0)

#define GLD16(dst, p, off) asm volatile("global_load_dwordx4 %0, %1, off offset:%2" : "=v"(dst) : "v"(p), "i"(off) : "memory")
#define GLD4(dst, p) asm volatile("global_load_dword %0, %1, off" : "=v"(dst) : "v"(p) : "memory")
#define GLD2(dst, p, off) asm volatile("global_load_ushort %0, %1, off offset:%2" : "=v"(dst) : "v"(p), "i"(off) : "memory")
__device__ __forceinline__ void glds16(const void* gsrc, unsigned lds_dst) {
  unsigned keep;
  asm volatile("s_mov_b32 %0, m0\n\ts_mov_b32 m0, %2\n\ts_nop 0\n\tglobal_load_lds_dwordx4 %1, off\n\ts_mov_b32 m0, %0"
               : "=&s"(keep) : "v"(gsrc), "s"(lds_dst) : "memory");
}

#define STAMP(i)
constexpr size_t OFF_DBG = (size_t)64 << 20;

struct K1Args {
  const u16 *x, *Lre, *Lim, *Bre, *Bim, *Cre, *Cim, *ldt, *wg, *wu, *wd;
  char* ws;
};

template <bool GT>
__device__ __forceinline__ void k1_body(const K1Args& A, char* smem, int pi, int nb) {
  const int tid = threadIdx.x, lane = tid & 63;
  const int w = __builtin_amdgcn_readfirstlane(tid >> 6);
  const int t16 = lane & 15, q = lane >> 4;
  constexpr int NT = GT ? 1 : 9;
  const int sig0 = GT ? nb : 28 + 9 * (nb - 28);
  const int blk = pi * 32 + nb;
  STAMP(0);
  LAS char* lds = (LAS char*)smem;
  const unsigned lds0 = (unsigned)(uintptr_t)lds;

  unsigned sw[8];
  unsigned sc[4];
  const int wid_ = blk * 7 + (w < 7 ? w : 0);
  const int wdt = wid_ & 31, wks = wid_ >> 5;
  {
    const u16* p = A.wd + (size_t)(32 * wks + 4 * q) * D_ + 16 * wdt + t16;
    GLD2(sw[0], p, 0); GLD2(sw[1], p, 1024); GLD2(sw[2], p, 2048); GLD2(sw[3], p, 3072);
    const u16* p2 = p + 16 * D_;
    GLD2(sw[4], p2, 0); GLD2(sw[5], p2, 1024); GLD2(sw[6], p2, 2048); GLD2(sw[7], p2, 3072);
  }
  const int cid_ = blk * 4 + (w & 3);
  const int cdt = cid_ & 31, cks = cid_ >> 5;
  {
    const size_t o = (size_t)(16 * cdt + t16) * 512 + 16 * cks + 2 * q;
    GLD4(sc[0], A.Cre + o); GLD4(sc[1], A.Cim + o); GLD4(sc[2], A.Cre + o + 8); GLD4(sc[3], A.Cim + o + 8);
  }
  unsigned plr[2], pli[2], pdt[2];
  {
    const int i0 = GT ? 0 : w;
    const int c0 = 8 * (sig0 + i0) + 2 * q;
    GLD4(plr[0], A.Lre + c0); GLD4(pli[0], A.Lim + c0); GLD4(pdt[0], A.ldt + c0);
    if (!GT) {
      const int c1 = 8 * (sig0 + 8) + 2 * q;
      GLD4(plr[1], A.Lre + c1); GLD4(pli[1], A.Lim + c1); GLD4(pdt[1], A.ldt + c1);
    }
  }
  bf16x8 xf[2][16];
  const u16* xp0 = A.x + (size_t)(pi * 256 + 32 * w + t16) * D_ + 8 * q;
  const u16* xp1 = xp0 + 16 * D_;
  {
    const int r8 = lane >> 3, pc = lane & 7;
    auto ssm_src = [&](int ti, int jj, int c) -> const u16* {
      const int n = 8 * jj + r8;
      const int piece = pc ^ ((n >> 1) & 7);
      const u16* base = (n & 2) ? A.Bim : A.Bre;
      return base + (size_t)(8 * (sig0 + ti) + 2 * (n >> 2) + (n & 1)) * 512 + 64 * c + 8 * piece;
    };
    {
      const int ti = GT ? 0 : 8;
      glds16(ssm_src(ti, 0, w), lds0 + w * 18432 + 16384);
      glds16(ssm_src(ti, 1, w), lds0 + w * 18432 + 17408);
    }
    const u16 *s0, *s1;
    size_t step;
    if (GT) {
      const int r = 8 * w + r8;
      const int f = ((r >> 1) & 1) | (((r >> 3) & 1) << 1);
      const int piece = pc ^ (f << 1);
      s0 = A.wg + (size_t)r * F_ + 64 * nb + 8 * piece;
      s1 = A.wu + (size_t)r * F_ + 64 * nb + 8 * piece;
      step = (size_t)64 * F_;
    } else {
      s0 = ssm_src(w >> 1, w & 1, 0);
      s1 = ssm_src(4 + (w >> 1), w & 1, 0);
      step = 64;
    }
#pragma unroll
    for (int c = 0; c < 8; ++c) {
      glds16(s0 + c * step, lds0 + c * 18432 + w * 1024);
      glds16(s1 + c * step, lds0 + c * 18432 + 8192 + w * 1024);
      if (c < 4) {
        GLD16(xf[0][2 * c], xp0, 128 * c);
        GLD16(xf[1][2 * c], xp1, 128 * c);
        GLD16(xf[0][2 * c + 1], xp0, 128 * c + 64);
        GLD16(xf[1][2 * c + 1], xp1, 128 * c + 64);
      }
    }
  }
  STAMP(1);
  if (GT) {
    asm volatile("s_waitcnt vmcnt(34)"
                 : "+v"(sw[0]), "+v"(sw[1]), "+v"(sw[2]), "+v"(sw[3]), "+v"(sw[4]), "+v"(sw[5]), "+v"(sw[6]), "+v"(sw[7]),
                   "+v"(sc[0]), "+v"(sc[1]), "+v"(sc[2]), "+v"(sc[3]), "+v"(plr[0]), "+v"(pli[0]), "+v"(pdt[0])::"memory");
  } else {
    asm volatile("s_waitcnt vmcnt(34)"
                 : "+v"(sw[0]), "+v"(sw[1]), "+v"(sw[2]), "+v"(sw[3]), "+v"(sw[4]), "+v"(sw[5]), "+v"(sw[6]), "+v"(sw[7]),
                   "+v"(sc[0]), "+v"(sc[1]), "+v"(sc[2]), "+v"(sc[3]), "+v"(plr[0]), "+v"(pli[0]), "+v"(pdt[0]),
                   "+v"(plr[1]), "+v"(pli[1]), "+v"(pdt[1])::"memory");
  }
  STAMP(2);
  u32x4 swp = {sw[0] | (sw[1] << 16), sw[2] | (sw[3] << 16), sw[4] | (sw[5] << 16), sw[6] | (sw[7] << 16)};
  u32x4 scp = {sc[0], sc[1] ^ 0x80008000u, sc[2], sc[3] ^ 0x80008000u};
  asm volatile("" : "+v"(swp), "+v"(scp));
  {
    const int nround = GT ? 1 : 2;
#pragma unroll
    for (int rd = 0; rd < nround; ++rd) {
      const bool act = GT ? (w == 0) : (rd == 0 || w == 0);
      if (act) {
        const int ti = GT ? 0 : (rd == 0 ? w : 8);
        f32x4 lam = {bf2f(plr[rd] & 0xffff), bf2f(plr[rd] >> 16), bf2f(pli[rd] & 0xffff), bf2f(pli[rd] >> 16)};
        const float dt0 = rbf(expf(bf2f(pdt[rd] & 0xffff))), dt1 = rbf(expf(bf2f(pdt[rd] >> 16)));
        float s0, c0, s1, c1;
        sincosf(dt0 * lam[2], &s0, &c0);
        sincosf(dt1 * lam[3], &s1, &c1);
        const float e0 = expf(dt0 * lam[0]), e1 = expf(dt1 * lam[1]);
        f32x4 a = {e0 * c0, e1 * c1, e0 * s0, e1 * s1};
        const int n = 15 - t16;
        f32x4 one = {1.f, 1.f, 0.f, 0.f};
        f32x4 p = (n & 1) ? a : one;
        f32x4 a2 = cmul(a, a);
        p = cmul(p, (n & 2) ? a2 : one);
        f32x4 a4 = cmul(a2, a2);
        p = cmul(p, (n & 4) ? a4 : one);
        f32x4 a8 = cmul(a4, a4);
        p = cmul(p, (n & 8) ? a8 : one);
        *(LAS f32x4*)(lds + L1_PW + (ti * 64 + lane) * 16) = p;
        if (t16 == 0) *(LAS f32x4*)(lds + L1_LAM + (ti * 4 + q) * 16) = lam;
      }
    }
  }
  STAMP(3);

  f32x4 acc[9][2];
#pragma unroll
  for (int i = 0; i < 9; ++i) { acc[i][0] = (f32x4){0.f, 0.f, 0.f, 0.f}; acc[i][1] = (f32x4){0.f, 0.f, 0.f, 0.f}; }
  f32x4 csw = {0.f, 0.f, 0.f, 0.f}, cs8 = {0.f, 0.f, 0.f, 0.f};
  float s1[2] = {0.f, 0.f}, s2[2] = {0.f, 0.f};
  const unsigned ONES = 0x3F803F80u;
  const bf16x8 onesf = __builtin_bit_cast(bf16x8, (u32x4){ONES, ONES, ONES, ONES});
  const int qq = t16 >> 2, pp = t16 & 3;
  const int fsw = (qq >> 1) | ((q & 1) << 1);
  const int trbase = (8 * q + qq) * 128 + 32 * fsw + 8 * pp;
  const int rbase = t16 * 128 + 16 * (q ^ ((t16 >> 1) & 7));
  const int cwbase = GT ? ((w >> 2) * 8192 + (trbase ^ ((w & 3) << 5))) : (w * 2048);

  auto read_frags = [&](bf16x8(&wf)[10], int ks) {
    const int c = ks >> 1, kk = ks & 1;
#pragma unroll
    for (int i = 0; i < 10; ++i) {
      if (GT && i != 8) {
        LAS char* p = lds + c * 18432 + kk * 4096 + (i == 9 ? cwbase : ((i >> 2) * 8192 + (trbase ^ ((i & 3) << 5))));
        s16x4 lo = __builtin_amdgcn_ds_read_tr16_b64_v4i16((LAS s16x4*)p);
        s16x4 hi = __builtin_amdgcn_ds_read_tr16_b64_v4i16((LAS s16x4*)(p + 512));
        wf[i] = __builtin_shufflevector(lo, hi, 0, 1, 2, 3, 4, 5, 6, 7);
      } else {
        wf[i] = *(LAS bf16x8*)(lds + c * 18432 + (i == 9 ? cwbase : i * 2048) + (rbase ^ (kk << 6)));
      }
    }
  };
  auto compute = [&](bf16x8(&wf)[10], int ks) {
#pragma unroll
    for (int i = 0; i < 9; ++i) {
      acc[i][0] = MFMA(wf[i], xf[0][ks], acc[i][0]);
      acc[i][1] = MFMA(wf[i], xf[1][ks], acc[i][1]);
    }
    csw = MFMA(wf[9], onesf, csw);
    if ((ks >> 1) == w) cs8 = MFMA(wf[8], onesf, cs8);
#pragma unroll
    for (int tt = 0; tt < 2; ++tt) {
      u32x4 xv = __builtin_bit_cast(u32x4, xf[tt][ks]);
#pragma unroll
      for (int j = 0; j < 4; ++j) { s1[tt] = dot2(xv[j], ONES, s1[tt]); s2[tt] = dot2(xv[j], xv[j], s2[tt]); }
    }
    asm volatile("" : "+v"(s1[0]), "+v"(s1[1]), "+v"(s2[0]), "+v"(s2[1]));
  };

  bf16x8 wfa[10], wfb[10];
#pragma unroll
  for (int c = 0; c < 8; ++c) {
    asm volatile("s_waitcnt vmcnt(%4)" : "+v"(xf[0][2 * c]), "+v"(xf[1][2 * c]), "+v"(xf[0][2 * c + 1]), "+v"(xf[1][2 * c + 1]) : "i"(c < 4 ? 26 - 2 * c : 28 - 4 * c) : "memory");
    __builtin_amdgcn_s_barrier();
    asm volatile("" ::: "memory");
    STAMP(4 + c);
    read_frags(wfa, 2 * c);
    read_frags(wfb, 2 * c + 1);
    compute(wfa, 2 * c);
    compute(wfb, 2 * c + 1);
    if (c < 4) {
      GLD16(xf[0][2 * c + 8], xp0, 128 * c + 512);
      GLD16(xf[1][2 * c + 8], xp1, 128 * c + 512);
      GLD16(xf[0][2 * c + 9], xp0, 128 * c + 576);
      GLD16(xf[1][2 * c + 9], xp1, 128 * c + 576);
    }
  }
  if (t16 == 0) {
    *(LAS f32x4*)(lds + L1_CS + (16 * w + 4 * q) * 4) = csw;
    *(LAS f32x4*)(lds + L1_CS8 + (16 * w + 4 * q) * 4) = cs8;
  }
  asm volatile("s_waitcnt lgkmcnt(0)" ::: "memory");
  __builtin_amdgcn_s_barrier();
  asm volatile("" ::: "memory");
  STAMP(12);

  char* ws = A.ws;
  f32x4 cs8s = {0.f, 0.f, 0.f, 0.f};
#pragma unroll
  for (int i = 0; i < 8; ++i) cs8s += *(LAS f32x4*)(lds + L1_CS8 + (16 * i + 4 * q) * 4);
#pragma unroll
  for (int tt = 0; tt < 2; ++tt) {
    float sum = s1[tt], sq = s2[tt];
    sum += __shfl_xor(sum, 16); sum += __shfl_xor(sum, 32);
    sq += __shfl_xor(sq, 16); sq += __shfl_xor(sq, 32);
    const float mean = sum * (1.f / 512.f);
    const float var = fmaxf(sq * (1.f / 512.f) - mean * mean, 0.f);
    const float mb = rbf(mean);
    const float sd = rbf(sqrtf(rbf(rbf(var) + 1e-5f)));
    const float rs = 1.f / sd;
    const int tau = pi * 16 + 2 * w + tt;
    if (nb == 0 && q == 0) *(f32x2*)(ws + OFF_ST + (size_t)(tau * 16 + t16) * 8) = (f32x2){mb, sd};
    if (GT) {
      const float rsg = rs * -1.44269504088896f;
#pragma unroll
      for (int pr = 0; pr < 2; ++pr) {
        u32x4 o;
#pragma unroll
        for (int e = 0; e < 2; ++e) {
          const int i = 2 * pr + e;
          f32x4 cg = *(LAS f32x4*)(lds + L1_CS + (16 * i + 4 * q) * 4);
          f32x4 cu = *(LAS f32x4*)(lds + L1_CS + (64 + 16 * i + 4 * q) * 4);
          float hv[4];
#pragma unroll
          for (int r = 0; r < 4; ++r) {
            const float g = fmaf(-mb, cg[r], acc[i][tt][r]) * rsg;
            const float u = fmaf(-mb, cu[r], acc[4 + i][tt][r]) * rs;
            hv[r] = u * __builtin_amdgcn_rcpf(1.f + __builtin_amdgcn_exp2f(g));
          }
          o[2 * e] = pk2(hv[0], hv[1]);
          o[2 * e + 1] = pk2(hv[2], hv[3]);
        }
        *(u32x4*)(ws + OFF_A + ((size_t)((tau >> 2) * KS_TOT + 2 * nb + pr) * 4 + (tau & 3)) * 1024 + lane * 16) = o;
      }
    }
#pragma unroll
    for (int ti = 0; ti < NT; ++ti) {
      const int i = GT ? 8 : ti;
      f32x4 cs = (i == 8) ? cs8s : *(LAS f32x4*)(lds + L1_CS + (16 * i + 4 * q) * 4);
      f32x4 v;
#pragma unroll
      for (int r = 0; r < 4; ++r) v[r] = fmaf(-mb, cs[r], acc[i][tt][r]) * rs;
      const int sig = sig0 + ti;
      *(f32x4*)(ws + OFF_V + (size_t)(tau * 64 + sig) * 1024 + lane * 16) = v;
      f32x4 pw = *(LAS f32x4*)(lds + L1_PW + (ti * 64 + lane) * 16);
      f32x4 z = cmul(pw, v);
#pragma unroll
      for (int r = 0; r < 4; ++r) {
        z[r] += dpp0<0x111>(z[r]);
        z[r] += dpp0<0x112>(z[r]);
        z[r] += dpp0<0x114>(z[r]);
        z[r] += dpp0<0x118>(z[r]);
      }
      if (t16 == 15) *(f32x4*)(ws + OFF_AGG + ((size_t)(tau * 64 + sig) * 4 + q) * 16) = z;
    }
  }
  if (w < 7) *(u32x4*)(ws + OFF_B + ((size_t)((wdt >> 2) * KS_TOT + wks) * 4 + (wdt & 3)) * 1024 + lane * 16) = swp;
  if (w < 4) *(u32x4*)(ws + OFF_B + ((size_t)((cdt >> 2) * KS_TOT + 56 + cks) * 4 + (cdt & 3)) * 1024 + lane * 16) = scp;
  if (pi == 0 && tid < NT * 4) {
    const int ti = tid >> 2, qx = tid & 3;
    LAS f32x4* pt = (LAS f32x4*)(lds + L1_PW + (ti * 64 + qx * 16) * 16);
    f32x4 a = pt[14], a2 = pt[13], a4 = pt[11], a8 = pt[7];
    f32x4 lam = *(LAS f32x4*)(lds + L1_LAM + (ti * 4 + qx) * 16);
    f32x4 a16 = cmul(a8, a8);
    f32x4 a32 = cmul(a16, a16), a64 = cmul(a32, a32), a128 = cmul(a64, a64);
    f32x4 cf;
#pragma unroll
    for (int e = 0; e < 2; ++e) {
      const float nr = a[e] - 1.f, ni = a[2 + e], lr = lam[e], li = lam[2 + e];
      const float den = lr * lr + li * li;
      cf[e] = (nr * lr + ni * li) / den;
      cf[2 + e] = (ni * lr - nr * li) / den;
    }
    f32x4* ct = (f32x4*)(ws + OFF_CT + (size_t)((sig0 + ti) * 4 + qx) * 128);
    ct[0] = a; ct[1] = a2; ct[2] = a4; ct[3] = a8; ct[4] = a16; ct[5] = a128; ct[6] = cf;
  }
  if (blk == 0 && tid < 32) *(unsigned*)(ws + OFF_FL + tid * 128) = 0u;
  STAMP(13);
}

__global__ __launch_bounds__(512, 2) void k1(K1Args A) {
  extern __shared__ __attribute__((aligned(16))) char smem[];
  const int b = blockIdx.x;
  const int xcd = b & 7, r = b >> 3;
  const int pi = (xcd & 1) * 4 + (r & 3), nb = (xcd >> 1) * 8 + (r >> 2);
  if (nb < 28) k1_body<true>(A, smem, pi, nb);
  else k1_body<false>(A, smem, pi, nb);
}

struct K2Args {
  const u16 *x, *Dk;
  char* ws;
  float* out;
};

#define SBAR() __builtin_amdgcn_sched_barrier(0)

__global__ __launch_bounds__(512, 2) void k2(K2Args A) {
  extern __shared__ __attribute__((aligned(16))) char smem[];
  LAS char* lds = (LAS char*)smem;
  const int tid = threadIdx.x, lane = tid & 63;
  const int w = __builtin_amdgcn_readfirstlane(tid >> 6);
  const int t16 = lane & 15, q = lane >> 4;
  const int b = blockIdx.x;
  const int xcd = b & 7, r = b >> 3;
  const int mi = xcd * 4 + (r >> 3), nj = r & 7;
  char* ws = A.ws;
  STAMP(0);

  const bf16x8* Apg = (const bf16x8*)(ws + OFF_A) + (size_t)(mi * KS_TOT + 7 * w) * 256 + lane;
  const bf16x8* Bpg = (const bf16x8*)(ws + OFF_B) + (size_t)(nj * KS_TOT + 7 * w) * 256 + lane;
  const bf16x8* Apy = (const bf16x8*)(ws + OFF_A) + (size_t)(mi * KS_TOT + 56 + 4 * w) * 256 + lane;
  const bf16x8* Bpy = (const bf16x8*)(ws + OFF_B) + (size_t)(nj * KS_TOT + 56 + 4 * w) * 256 + lane;
  bf16x8 af[11][4], bfr[11][4];
  auto issue = [&](int s) {
    const bf16x8* ap = s < 7 ? Apg + s * 256 : Apy + (s - 7) * 256;
    const bf16x8* bp = s < 7 ? Bpg + s * 256 : Bpy + (s - 7) * 256;
#pragma unroll
    for (int i = 0; i < 4; ++i) { bfr[s][i] = bp[i * 64]; af[s][i] = ap[i * 64]; }
  };

  {
    const int tau0 = 4 * mi;
    const int grp = tid & 31, part = tid >> 5;
    const f32x4* agg = (const f32x4*)(ws + OFF_AGG) + (size_t)(8 * nj) * 4 + grp;
    const f32x4* ctg = (const f32x4*)(ws + OFF_CT) + (size_t)(32 * nj + grp) * 8;
    f32x4 ag[8];
#pragma unroll
    for (int i = 0; i < 8; ++i) {
      const int tp = tau0 - 1 - 8 * part - i;
      ag[i] = agg[(size_t)(tp < 0 ? 0 : tp) * 256];
    }
    const f32x4 a16 = ctg[4], a128 = ctg[5];
    f32x4 agn[3];
#pragma unroll
    for (int i = 0; i < 3; ++i) agn[i] = agg[(size_t)(tau0 + i) * 256];
    const int tl = w >> 1;
    f32x4 vv[2][2];
#pragma unroll
    for (int it = 0; it < 2; ++it)
#pragma unroll
      for (int e = 0; e < 2; ++e) {
        const int sl = 2 * (2 * (w & 1) + it) + e;
        vv[it][e] = *(const f32x4*)(ws + OFF_V + (size_t)((tau0 + tl) * 64 + 8 * nj + sl) * 1024 + lane * 16);
      }
    SBAR();
    issue(0); issue(1);
    SBAR();
    const f32x4 zero = {0.f, 0.f, 0.f, 0.f};
    f32x4 P = zero;
#pragma unroll
    for (int i = 7; i >= 0; --i) {
      const int tp = tau0 - 1 - 8 * part - i;
      P = cfma(P, a16, tp < 0 ? zero : ag[i]);
    }
    {
      const f32x4 one = {1.f, 1.f, 0.f, 0.f};
      f32x4 pw = (part & 1) ? a128 : one;
      f32x4 s = cmul(a128, a128);
      pw = cmul(pw, (part & 2) ? s : one);
      s = cmul(s, s);
      pw = cmul(pw, (part & 4) ? s : one);
      s = cmul(s, s);
      pw = cmul(pw, (part & 8) ? s : one);
      P = (tau0 - 1 - 8 * part < 0) ? zero : cmul(P, pw);
    }
    LAS f32x4* sp = (LAS f32x4*)lds;
    LAS f32x4* sc = (LAS f32x4*)(lds + 8192);
    sp[part * 32 + grp] = P;
    f32x4 ca[2][2][4], cf[2][2];
#pragma unroll
    for (int it = 0; it < 2; ++it)
#pragma unroll
      for (int e = 0; e < 2; ++e) {
        const int sl = 2 * (2 * (w & 1) + it) + e;
        const f32x4* ct = (const f32x4*)(ws + OFF_CT) + (size_t)((8 * nj + sl) * 4 + q) * 8;
#pragma unroll
        for (int k = 0; k < 4; ++k) ca[it][e][k] = ct[k];
        cf[it][e] = ct[6];
      }
    STAMP(1);
    __syncthreads();
    if (tid < 32) {
      f32x4 c0 = zero;
#pragma unroll
      for (int i = 0; i < 16; ++i) c0 += sp[i * 32 + tid];
      sc[tid] = c0;
      f32x4 c1 = cfma(c0, a16, agn[0]); sc[32 + tid] = c1;
      f32x4 c2 = cfma(c1, a16, agn[1]); sc[64 + tid] = c2;
      f32x4 c3 = cfma(c2, a16, agn[2]); sc[96 + tid] = c3;
    }
    __syncthreads();
    STAMP(2);
#pragma unroll
    for (int it = 0; it < 2; ++it) {
      u32x4 o;
#pragma unroll
      for (int e = 0; e < 2; ++e) {
        const int sl = 2 * (2 * (w & 1) + it) + e;
        f32x4 c = sc[tl * 32 + sl * 4 + q];
        f32x4 h = vv[it][e];
        h = cfma(ca[it][e][0], (t16 == 0) ? c : zero, h);
        {
          f32x4 s;
#pragma unroll
          for (int rr = 0; rr < 4; ++rr) s[rr] = dpp0<0x111>(h[rr]);
          h = cfma(ca[it][e][0], s, h);
#pragma unroll
          for (int rr = 0; rr < 4; ++rr) s[rr] = dpp0<0x112>(h[rr]);
          h = cfma(ca[it][e][1], s, h);
#pragma unroll
          for (int rr = 0; rr < 4; ++rr) s[rr] = dpp0<0x114>(h[rr]);
          h = cfma(ca[it][e][2], s, h);
#pragma unroll
          for (int rr = 0; rr < 4; ++rr) s[rr] = dpp0<0x118>(h[rr]);
          h = cfma(ca[it][e][3], s, h);
        }
        h = cmul(h, cf[it][e]);
        o[2 * e] = pk2(h[0], h[1]);
        o[2 * e + 1] = pk2(h[2], h[3]);
      }
      const int ksl = 2 * (w & 1) + it;
      u32x4* dst = (u32x4*)(ws + OFF_A + ((size_t)(mi * KS_TOT + 56 + 4 * nj + ksl) * 4 + tl) * 1024 + lane * 16);
      asm volatile("global_store_dwordx4 %0, %1, off sc0 sc1\n\ts_nop 1" ::"v"(dst), "v"(o) : "memory");
    }
    asm volatile("s_waitcnt vmcnt(0)" ::: "memory");
    __syncthreads();
    if (tid == 0) __hip_atomic_fetch_add((unsigned*)(ws + OFF_FL + mi * 128), 1u, __ATOMIC_RELAXED, __HIP_MEMORY_SCOPE_AGENT);
  }
  STAMP(3);
  SBAR();
  issue(2); issue(3); issue(4);
  SBAR();

  f32x4 accg[4][4], accy[4][4];
#pragma unroll
  for (int i = 0; i < 4; ++i)
#pragma unroll
    for (int j = 0; j < 4; ++j) { accg[i][j] = (f32x4){0.f, 0.f, 0.f, 0.f}; accy[i][j] = (f32x4){0.f, 0.f, 0.f, 0.f}; }
  auto mm = [&](f32x4(&acc)[4][4], int s) {
#pragma unroll
    for (int dt = 0; dt < 4; ++dt)
#pragma unroll
      for (int tt = 0; tt < 4; ++tt) acc[dt][tt] = MFMA(bfr[s][dt], af[s][tt], acc[dt][tt]);
  };
  mm(accg, 0); SBAR(); issue(5); SBAR();
  mm(accg, 1); SBAR(); issue(6); SBAR();
  mm(accg, 2); SBAR();
  STAMP(4);
  if (tid == 0) {
    unsigned* fl = (unsigned*)(ws + OFF_FL + mi * 128);
    unsigned spins = 0;
    while (__hip_atomic_load(fl, __ATOMIC_RELAXED, __HIP_MEMORY_SCOPE_AGENT) < 8u) {
      __builtin_amdgcn_s_sleep(1);
      if (++spins > (1u << 24)) break;
    }
    __builtin_amdgcn_fence(__ATOMIC_ACQUIRE, "agent");
    asm volatile("s_waitcnt vmcnt(0)" ::: "memory");
  }
  __syncthreads();
  STAMP(5);
  SBAR(); issue(7); SBAR();
  mm(accg, 3); SBAR(); issue(8); SBAR();
  mm(accg, 4); SBAR(); issue(9); SBAR();
  mm(accg, 5); SBAR(); issue(10); SBAR();
  mm(accg, 6); SBAR();
  STAMP(6);
  LAS f32x4* red = (LAS f32x4*)lds;
  const int ott = w >> 1, od0 = 2 * (w & 1);
  f32x4 G[2], Y[2];
#pragma unroll
  for (int dt = 0; dt < 4; ++dt)
#pragma unroll
    for (int tt = 0; tt < 4; ++tt) red[(w * 16 + dt * 4 + tt) * 64 + lane] = accg[dt][tt];
  SBAR();
  mm(accy, 7); SBAR();
  __syncthreads();
#pragma unroll
  for (int e = 0; e < 2; ++e) {
    f32x4 s = {0.f, 0.f, 0.f, 0.f};
#pragma unroll
    for (int ww = 0; ww < 8; ++ww) s += red[(ww * 16 + (od0 + e) * 4 + ott) * 64 + lane];
    G[e] = s;
  }
  SBAR();
  mm(accy, 8); SBAR();
  mm(accy, 9); SBAR();
  mm(accy, 10); SBAR();
  STAMP(7);
  __syncthreads();
#pragma unroll
  for (int dt = 0; dt < 4; ++dt)
#pragma unroll
    for (int tt = 0; tt < 4; ++tt) red[(w * 16 + dt * 4 + tt) * 64 + lane] = accy[dt][tt];
  const int row = 64 * mi + 16 * ott + t16;
  const f32x2 st = *(const f32x2*)(ws + OFF_ST + (size_t)row * 8);
  u32x2 xv[2], dv[2];
#pragma unroll
  for (int e = 0; e < 2; ++e) {
    const int d0 = 64 * nj + 16 * (od0 + e) + 4 * q;
    xv[e] = *(const u32x2*)(A.x + (size_t)row * D_ + d0);
    dv[e] = *(const u32x2*)(A.Dk + d0);
  }
  __syncthreads();
  STAMP(8);
#pragma unroll
  for (int e = 0; e < 2; ++e) {
    f32x4 s = {0.f, 0.f, 0.f, 0.f};
#pragma unroll
    for (int ww = 0; ww < 8; ++ww) s += red[(ww * 16 + (od0 + e) * 4 + ott) * 64 + lane];
    Y[e] = s;
  }
#pragma unroll
  for (int e = 0; e < 2; ++e) {
    const int d0 = 64 * nj + 16 * (od0 + e) + 4 * q;
    f32x4 o;
#pragma unroll
    for (int rr = 0; rr < 4; ++rr) {
      const float xx = bf2f((xv[e][rr >> 1] >> (16 * (rr & 1))) & 0xffff);
      const float dd = bf2f((dv[e][rr >> 1] >> (16 * (rr & 1))) & 0xffff);
      const float xn = rbf(rbf(xx - st[0]) / st[1]);
      const float y = 2.f * Y[e][rr] + rbf(xn * dd);
      const float g = rbf(G[e][rr]);
      const float sg = rbf(g / (1.f + __expf(-g)));
      o[rr] = xx + y * sg;
    }
    *(f32x4*)(A.out + (size_t)row * D_ + d0) = o;
  }
  STAMP(9);
}

extern "C" void kernel_launch(void* const* d_in, const int* in_sizes, int n_in, void* d_out, int out_size, void* d_ws,
                              size_t ws_size, hipStream_t stream) {
  K1Args a;
  a.x = (const u16*)d_in[0]; a.Lre = (const u16*)d_in[1]; a.Lim = (const u16*)d_in[2];
  a.Bre = (const u16*)d_in[3]; a.Bim = (const u16*)d_in[4]; a.Cre = (const u16*)d_in[5]; a.Cim = (const u16*)d_in[6];
  a.ldt = (const u16*)d_in[7]; a.wg = (const u16*)d_in[10]; a.wu = (const u16*)d_in[11]; a.wd = (const u16*)d_in[12];
  a.ws = (char*)d_ws;
  (void)hipFuncSetAttribute((const void*)k1, hipFuncAttributeMaxDynamicSharedMemorySize, L1_SIZE);
  (void)hipFuncSetAttribute((const void*)k2, hipFuncAttributeMaxDynamicSharedMemorySize, 131072);
  k1<<<256, 512, L1_SIZE, stream>>>(a);
  K2Args b;
  b.x = (const u16*)d_in[0]; b.Dk = (const u16*)d_in[8]; b.ws = (char*)d_ws; b.out = (float*)d_out;
  k2<<<256, 512, 131072, stream>>>(b);
}
```

```cpp
#include <hip/hip_runtime.h>
#include <hip/hip_bf16.h>
#include <stdint.h>

typedef unsigned short u16;
typedef __attribute__((ext_vector_type(8))) short bf16x8;
typedef __attribute__((ext_vector_type(4))) short s16x4;
typedef __attribute__((ext_vector_type(4))) float f32x4;
typedef __attribute__((ext_vector_type(2))) float f32x2;
typedef __attribute__((ext_vector_type(4))) unsigned u32x4;
typedef __attribute__((ext_vector_type(2))) unsigned u32x2;
typedef __attribute__((ext_vector_type(2))) __bf16 bf16x2_t;
#define LAS __attribute__((address_space(3)))

constexpr int S_ = 2048, D_ = 512, F_ = 1792;
constexpr int KS_TOT = 88;

constexpr size_t OFF_A   = 0;
constexpr size_t OFF_V   = OFF_A + (size_t)32 * 88 * 4096;
constexpr size_t OFF_CAR = OFF_V + (size_t)128 * 64 * 1024;
constexpr size_t OFF_TOT = OFF_CAR + (size_t)128 * 256 * 16;
constexpr size_t OFF_ST  = OFF_TOT + (size_t)8 * 256 * 16;
constexpr size_t OFF_CT  = OFF_ST + (size_t)2048 * 8;
constexpr size_t OFF_FL  = OFF_CT + (size_t)3 * 256 * 16;

constexpr int L1_XR   = 0;
constexpr int L1_GR   = 98304;
constexpr int L1_SS   = 131072;
constexpr int L1_PW   = 147456;
constexpr int L1_AC   = L1_PW + 9216;
constexpr int L1_CS   = L1_AC + 1152;
constexpr int L1_SIZE = L1_CS + 1536;
constexpr int L1_AGG  = 32768;

__device__ __forceinline__ float bf2f(unsigned h) { return __builtin_bit_cast(float, h << 16); }
__device__ __forceinline__ float rbf(float v) { return (float)(__bf16)v; }
__device__ __forceinline__ unsigned pk2(float lo, float hi) {
  bf16x2_t r = {(__bf16)lo, (__bf16)hi};
  return __builtin_bit_cast(unsigned, r);
}
template <int CTRL> __device__ __forceinline__ float dpp0(float x) {
  return __builtin_bit_cast(float, __builtin_amdgcn_update_dpp(0, __builtin_bit_cast(int, x), CTRL, 0xf, 0xf, true));
}
template <int CTRL> __device__ __forceinline__ f32x4 dpp4(f32x4 x) {
  f32x4 r;
#pragma unroll
  for (int i = 0; i < 4; ++i) r[i] = dpp0<CTRL>(x[i]);
  return r;
}
__device__ __forceinline__ f32x4 cmul(f32x4 a, f32x4 b) {
  f32x4 r;
  r[0] = a[0] * b[0] - a[2] * b[2];
  r[1] = a[1] * b[1] - a[3] * b[3];
  r[2] = a[0] * b[2] + a[2] * b[0];
  r[3] = a[1] * b[3] + a[3] * b[1];
  return r;
}
__device__ __forceinline__ f32x4 cfma(f32x4 a, f32x4 b, f32x4 c) {
  f32x4 r;
  r[0] = fmaf(a[0], b[0], fmaf(-a[2], b[2], c[0]));
  r[1] = fmaf(a[1], b[1], fmaf(-a[3], b[3], c[1]));
  r[2] = fmaf(a[0], b[2], fmaf(a[2], b[0], c[2]));
  r[3] = fmaf(a[1], b[3], fmaf(a[3], b[1], c[3]));
  return r;
}
#define MFMA(a, b, c) __builtin_amdgcn_mfma_f32_16x16x32_bf16(a, b, c, 0, 0, 0)

#define GLD16(dst, p) asm volatile("global_load_dwordx4 %0, %1, off" : "=v"(dst) : "v"(p) : "memory")
#define GLD8(dst, p) asm volatile("global_load_dwordx2 %0, %1, off" : "=v"(dst) : "v"(p) : "memory")
#define GLD4(dst, p) asm volatile("global_load_dword %0, %1, off" : "=v"(dst) : "v"(p) : "memory")
#define GST16WT(p, v) asm volatile("global_store_dwordx4 %0, %1, off sc0 sc1\n\ts_nop 1" ::"v"(p), "v"(v) : "memory")
#define VMCNT(n) asm volatile("s_waitcnt vmcnt(%0)" ::"i"(n) : "memory")
#define BARRIER() do { asm volatile("s_waitcnt lgkmcnt(0)" ::: "memory"); __builtin_amdgcn_s_barrier(); asm volatile("" ::: "memory"); } while (0)
__device__ __forceinline__ void glds16(const void* gsrc, unsigned lds_dst) {
  unsigned keep;
  asm volatile("s_mov_b32 %0, m0\n\ts_mov_b32 m0, %2\n\ts_nop 0\n\tglobal_load_lds_dwordx4 %1, off\n\ts_mov_b32 m0, %0"
               : "=&s"(keep) : "v"(gsrc), "s"(lds_dst) : "memory");
}

__device__ __forceinline__ void glds16c(const void* gsrc, unsigned lds_dst) {
  unsigned keep;
  asm volatile("s_mov_b32 %0, m0\n\ts_mov_b32 m0, %2\n\ts_nop 0\n\tglobal_load_lds_dwordx4 %1, off sc1\n\ts_mov_b32 m0, %0"
               : "=&s"(keep) : "v"(gsrc), "s"(lds_dst) : "memory");
}

#define STAMP(i)
constexpr size_t OFF_DBG = (size_t)64 << 20;

struct K1Args {
  const u16 *x, *Lre, *Lim, *Bre, *Bim, *Cre, *Cim, *ldt, *wg, *wu, *wd;
  char* ws;
};

template <bool GT>
__device__ __forceinline__ void k1_body(const K1Args& A, char* smem, int pi, int nb, int c0) {
  const int tid = threadIdx.x, lane = tid & 63;
  const int w = __builtin_amdgcn_readfirstlane(tid >> 6);
  const int t16 = lane & 15, q = lane >> 4;
  constexpr int NT = GT ? 1 : 9;
  const int sig0 = GT ? nb : 28 + 9 * (nb - 28);
  const int blk = pi * 32 + nb;
  STAMP(0);
  LAS char* lds = (LAS char*)smem;
  const unsigned lds0 = (unsigned)(uintptr_t)lds;
  const int r8 = lane >> 3, pc = lane & 7;

  unsigned plr = 0, pli = 0, pdt = 0;
  if (w == 0) {
    const int l = lane < NT * 4 ? lane : 0;
    const int sg = sig0 + (l >> 2);
    const int ch = 32 * (sg >> 2) + 8 * (l & 3) + 2 * (sg & 3);
    GLD4(plr, A.Lre + ch); GLD4(pli, A.Lim + ch); GLD4(pdt, A.ldt + ch);
  }
  auto ssm_src = [&](int ti, int jj, int c) -> const u16* {
    const int n = 8 * jj + r8;
    const int piece = pc ^ ((n >> 1) & 7);
    const int sg = sig0 + ti;
    const u16* base = (n & 2) ? A.Bim : A.Bre;
    return base + (size_t)(32 * (sg >> 2) + 8 * (n >> 2) + 2 * (sg & 3) + (n & 1)) * 512 + 64 * c + 8 * piece;
  };
  {
    const int c = (c0 + w) & 7;
    glds16(ssm_src(GT ? 0 : 8, 0, c), lds0 + L1_SS + w * 2048);
    glds16(ssm_src(GT ? 0 : 8, 1, c), lds0 + L1_SS + w * 2048 + 1024);
  }
  const u16* xsE = A.x + (size_t)(pi * 256 + 32 * w + r8) * D_ + 8 * (pc ^ (r8 >> 1));
  const u16* xsO = A.x + (size_t)(pi * 256 + 32 * w + 8 + r8) * D_ + 8 * (pc ^ (4 + (r8 >> 1)));
  const u16* gsrc;
  if (GT) {
    const int r = 8 * w + r8;
    const int f = ((r >> 1) & 1) | (((r >> 3) & 1) << 1);
    gsrc = A.wg + (size_t)r * F_ + 64 * nb + 8 * (pc ^ (f << 1));
  } else {
    gsrc = ssm_src(w >> 1, w & 1, 0);
  }
  auto issue_xg = [&](int i) {
    const int c = (c0 + i) & 7;
    const unsigned xd = lds0 + L1_XR + (i % 3) * 32768 + w * 4096;
    glds16(gsrc + (GT ? (size_t)c * 64 * F_ : (size_t)c * 64), lds0 + L1_GR + (i & 3) * 8192 + w * 1024);
    glds16(xsE + c * 64, xd);
    glds16(xsO + c * 64, xd + 1024);
    glds16(xsE + c * 64 + 16 * D_, xd + 2048);
    glds16(xsO + c * 64 + 16 * D_, xd + 3072);
  };
  auto issue_up = [&](int half) {
    const int c = (c0 + 4 * half + (w >> 1)) & 7;
    const unsigned ud = lds0 + L1_XR + (half ? 0 : 65536) + w * 4096;
#pragma unroll
    for (int d = 0; d < 4; ++d) {
      const u16* s;
      if (GT) {
        const int r = 32 * (w & 1) + 8 * d + r8;
        const int f = ((r >> 1) & 1) | (((r >> 3) & 1) << 1);
        s = A.wu + (size_t)(64 * c + r) * F_ + 64 * nb + 8 * (pc ^ (f << 1));
      } else {
        s = ssm_src(4 + 2 * (w & 1) + (d >> 1), d & 1, c);
      }
      glds16(s, ud + d * 1024);
    }
  };
  issue_xg(0); issue_xg(1); issue_xg(2);
  STAMP(1);
  if (w == 0) {
    asm volatile("s_waitcnt vmcnt(17)" : "+v"(plr), "+v"(pli), "+v"(pdt)::"memory");
    f32x4 lam = {bf2f(plr & 0xffff), bf2f(plr >> 16), bf2f(pli & 0xffff), bf2f(pli >> 16)};
    const float dt0 = rbf(expf(bf2f(pdt & 0xffff))), dt1 = rbf(expf(bf2f(pdt >> 16)));
    float s0, k0, s1, k1;
    sincosf(dt0 * lam[2], &s0, &k0);
    sincosf(dt1 * lam[3], &s1, &k1);
    const float e0 = expf(dt0 * lam[0]), e1 = expf(dt1 * lam[1]);
    f32x4 a = {e0 * k0, e1 * k1, e0 * s0, e1 * s1};
    f32x4 cf;
#pragma unroll
    for (int e = 0; e < 2; ++e) {
      const float nr = a[e] - 1.f, ni = a[2 + e], lr = lam[e], li = lam[2 + e];
      const float den = lr * lr + li * li;
      cf[e] = (nr * lr + ni * li) / den;
      cf[2 + e] = (ni * lr - nr * li) / den;
    }
    if (lane < NT * 4) {
      *(LAS f32x4*)(lds + L1_AC + lane * 16) = a;
      *(LAS f32x4*)(lds + L1_AC + 576 + lane * 16) = cf;
    }
  }
  STAMP(2);

  const f32x4 zero4 = {0.f, 0.f, 0.f, 0.f};
  f32x4 acc1[5][2], gr[2], sm[2], csg = zero4;
#pragma unroll
  for (int i = 0; i < 5; ++i) { acc1[i][0] = zero4; acc1[i][1] = zero4; }
  gr[0] = gr[1] = sm[0] = sm[1] = zero4;
  const unsigned ONES = 0x3F803F80u;
  const bf16x8 onesf = __builtin_bit_cast(bf16x8, (u32x4){ONES, ONES, ONES, ONES});
  const int qq = t16 >> 2, pp = t16 & 3;
  const int fsw = (qq >> 1) | ((q & 1) << 1);
  const int trbase = (8 * q + qq) * 128 + 32 * fsw + 8 * pp;
  const int rbase = t16 * 128 + 16 * (q ^ ((t16 >> 1) & 7));
  auto wfrag = [&](int base, int t, int kk) -> bf16x8 {
    if (GT) {
      LAS char* p = lds + base + kk * 4096 + (trbase ^ (t << 5));
      s16x4 lo = __builtin_amdgcn_ds_read_tr16_b64_v4i16((LAS s16x4*)p);
      s16x4 hi = __builtin_amdgcn_ds_read_tr16_b64_v4i16((LAS s16x4*)(p + 512));
      return __builtin_shufflevector(lo, hi, 0, 1, 2, 3, 4, 5, 6, 7);
    } else {
      return *(LAS bf16x8*)(lds + base + t * 2048 + (rbase ^ (kk << 6)));
    }
  };
  bf16x8 xf[2][16];
#pragma unroll
  for (int i = 0; i < 8; ++i) {
    VMCNT(i < 6 ? 10 : (i == 6 ? 9 : 8));
    BARRIER();
    STAMP(3 + i);
    {
      LAS char* xp = lds + L1_XR + (i % 3) * 32768 + w * 4096;
      xf[0][2 * i] = *(LAS bf16x8*)(xp + rbase);
      xf[1][2 * i] = *(LAS bf16x8*)(xp + 2048 + rbase);
      xf[0][2 * i + 1] = *(LAS bf16x8*)(xp + (rbase ^ 64));
      xf[1][2 * i + 1] = *(LAS bf16x8*)(xp + 2048 + (rbase ^ 64));
    }
    const int gb = L1_GR + (i & 3) * 8192;
    bf16x8 wf[2][5], cw;
#pragma unroll
    for (int t = 0; t < 4; ++t) wf[0][t] = wfrag(gb, t, 0);
    wf[0][4] = *(LAS bf16x8*)(lds + L1_SS + i * 2048 + rbase);
    asm volatile("" : "+v"(xf[0][2 * i]), "+v"(xf[1][2 * i]), "+v"(xf[0][2 * i + 1]), "+v"(xf[1][2 * i + 1])::"memory");
    if (i < 5) issue_xg(i + 3);
    else if (i < 7) issue_up(i - 5);
    cw = wfrag(gb, w & 3, w >> 2);
#pragma unroll
    for (int kk = 0; kk < 2; ++kk)
#pragma unroll
      for (int tt = 0; tt < 2; ++tt) {
        gr[tt] = MFMA(xf[tt][2 * i + kk], xf[tt][2 * i + kk], gr[tt]);
        sm[tt] = MFMA(onesf, xf[tt][2 * i + kk], sm[tt]);
      }
    __builtin_amdgcn_sched_barrier(0);
#pragma unroll
    for (int t = 0; t < 5; ++t) {
      acc1[t][0] = MFMA(wf[0][t], xf[0][2 * i], acc1[t][0]);
      acc1[t][1] = MFMA(wf[0][t], xf[1][2 * i], acc1[t][1]);
      if (t < 4) wf[1][t] = wfrag(gb, t, 1);
      else wf[1][4] = *(LAS bf16x8*)(lds + L1_SS + i * 2048 + (rbase ^ 64));
      __builtin_amdgcn_sched_barrier(0);
    }
#pragma unroll
    for (int t = 0; t < 5; ++t) {
      acc1[t][0] = MFMA(wf[1][t], xf[0][2 * i + 1], acc1[t][0]);
      acc1[t][1] = MFMA(wf[1][t], xf[1][2 * i + 1], acc1[t][1]);
    }
    csg = MFMA(cw, onesf, csg);
    if (i == 1) {
#pragma unroll
      for (int rd = 0; rd < (GT ? 1 : 2); ++rd) {
        if (GT ? (w == 0) : (rd == 0 || w == 0)) {
          const int ti = GT ? 0 : (rd == 0 ? w : 8);
          const f32x4 a = *(LAS f32x4*)(lds + L1_AC + (ti * 4 + q) * 16);
          const int n = 15 - t16;
          const f32x4 one = {1.f, 1.f, 0.f, 0.f};
          f32x4 p = (n & 1) ? a : one;
          f32x4 a2 = cmul(a, a);
          p = cmul(p, (n & 2) ? a2 : one);
          f32x4 a4 = cmul(a2, a2);
          p = cmul(p, (n & 4) ? a4 : one);
          f32x4 a8 = cmul(a4, a4);
          p = cmul(p, (n & 8) ? a8 : one);
          *(LAS f32x4*)(lds + L1_PW + (ti * 64 + lane) * 16) = p;
        }
      }
    }
  }
  VMCNT(0);
  BARRIER();
  STAMP(11);

  auto ubase = [&](int j) -> int { return L1_XR + (j < 4 ? 65536 + j * 8192 : (j - 4) * 8192); };
  {
    f32x4 csu = zero4, css = zero4;
    const int ub = L1_XR + ((w >> 2) ? 0 : 65536);
#pragma unroll
    for (int j = 0; j < 4; ++j)
#pragma unroll
      for (int kk = 0; kk < 2; ++kk) csu = MFMA(wfrag(ub + j * 8192, w & 3, kk), onesf, csu);
#pragma unroll
    for (int kk = 0; kk < 2; ++kk) css = MFMA(*(LAS bf16x8*)(lds + L1_SS + w * 2048 + (rbase ^ (kk << 6))), onesf, css);
    if (t16 == 0) {
      *(LAS f32x4*)(lds + L1_CS + (16 * w + 4 * q) * 4) = csg;
      *(LAS f32x4*)(lds + L1_CS + 512 + (16 * w + 4 * q) * 4) = csu;
      *(LAS f32x4*)(lds + L1_CS + 1024 + (16 * w + 4 * q) * 4) = css;
    }
  }
  f32x4 acc2[4][2];
  auto utile = [&](int t) {
    acc2[t][0] = zero4; acc2[t][1] = zero4;
    bf16x8 f[4];
#pragma unroll
    for (int ks = 0; ks < 4; ++ks) f[ks] = wfrag(ubase(ks >> 1), t, ks & 1);
    __builtin_amdgcn_sched_barrier(0);
#pragma unroll
    for (int ks = 0; ks < 16; ++ks) {
      acc2[t][0] = MFMA(f[ks & 3], xf[0][ks], acc2[t][0]);
      acc2[t][1] = MFMA(f[ks & 3], xf[1][ks], acc2[t][1]);
      if (ks + 4 < 16) f[ks & 3] = wfrag(ubase((ks + 4) >> 1), t, ks & 1);
      __builtin_amdgcn_sched_barrier(0);
    }
  };
  utile(0);
  BARRIER();
  STAMP(12);
  char* ws = A.ws;
  float mbv[2], rsv[2];
#pragma unroll
  for (int tt = 0; tt < 2; ++tt) {
    float dg = pp == 0 ? gr[tt][0] : (pp == 1 ? gr[tt][1] : (pp == 2 ? gr[tt][2] : gr[tt][3]));
    const float sq = __shfl(dg, qq * 16 + t16);
    const float mean = sm[tt][0] * (1.f / 512.f);
    const float var = fmaxf(sq * (1.f / 512.f) - mean * mean, 0.f);
    const float mb = rbf(mean);
    const float sd = rbf(sqrtf(rbf(rbf(var) + 1e-5f)));
    mbv[tt] = mb;
    rsv[tt] = 1.f / sd;
    if (nb == 0 && q == 0) *(f32x2*)(ws + OFF_ST + (size_t)((pi * 16 + 2 * w + tt) * 16 + t16) * 8) = (f32x2){mb, sd};
  }
  auto colsum = [&](int region, int t) -> f32x4 {
    return *(LAS f32x4*)(lds + L1_CS + region * 512 + (16 * t + 4 * q) * 4) +
           *(LAS f32x4*)(lds + L1_CS + region * 512 + 256 + (16 * t + 4 * q) * 4);
  };
  auto ssm_epi = [&](const f32x4(&acc)[2], f32x4 cs, int ti) {
    const f32x4 cf = *(LAS f32x4*)(lds + L1_AC + 576 + (ti * 4 + q) * 16);
    const f32x4 pw = *(LAS f32x4*)(lds + L1_PW + (ti * 64 + lane) * 16);
#pragma unroll
    for (int tt = 0; tt < 2; ++tt) {
      f32x4 v;
#pragma unroll
      for (int r = 0; r < 4; ++r) v[r] = fmaf(-mbv[tt], cs[r], acc[tt][r]) * rsv[tt];
      v = cmul(v, cf);
      const int tau = pi * 16 + 2 * w + tt;
      char* dst = ws + OFF_V + (size_t)(tau * 64 + sig0 + ti) * 1024 + lane * 16;
      GST16WT(dst, v);
      f32x4 z = cmul(pw, v);
      z += dpp4<0x111>(z);
      z += dpp4<0x112>(z);
      z += dpp4<0x114>(z);
      z += dpp4<0x118>(z);
      if (t16 == 15) *(LAS f32x4*)(lds + L1_AGG + ti * 1024 + (q * 16 + 2 * w + tt) * 16) = z;
    }
  };
  {
    f32x4 css = zero4;
#pragma unroll
    for (int i = 0; i < 8; ++i) css += *(LAS f32x4*)(lds + L1_CS + 1024 + (16 * i + 4 * q) * 4);
    ssm_epi(acc1[4], css, GT ? 0 : 8);
  }
  if (GT) {
#pragma unroll
    for (int t = 0; t < 4; ++t) {
      const f32x4 cg = colsum(0, t);
#pragma unroll
      for (int tt = 0; tt < 2; ++tt) {
        const float rsg = rsv[tt] * -1.44269504088896f;
#pragma unroll
        for (int r = 0; r < 4; ++r) {
          const float g = fmaf(-mbv[tt], cg[r], acc1[t][tt][r]) * rsg;
          acc1[t][tt][r] = rsv[tt] * __builtin_amdgcn_rcpf(1.f + __builtin_amdgcn_exp2f(g));
        }
      }
    }
    auto up_epi = [&](int pr) {
      const f32x4 cu0 = colsum(1, 2 * pr), cu1 = colsum(1, 2 * pr + 1);
#pragma unroll
      for (int tt = 0; tt < 2; ++tt) {
        u32x4 o;
#pragma unroll
        for (int e = 0; e < 2; ++e) {
          const int t = 2 * pr + e;
          float hv[4];
#pragma unroll
          for (int r = 0; r < 4; ++r) hv[r] = fmaf(-mbv[tt], e ? cu1[r] : cu0[r], acc2[t][tt][r]) * acc1[t][tt][r];
          o[2 * e] = pk2(hv[0], hv[1]);
          o[2 * e + 1] = pk2(hv[2], hv[3]);
        }
        const int tau = pi * 16 + 2 * w + tt;
        char* dst = ws + OFF_A + ((size_t)((tau >> 2) * KS_TOT + 2 * nb + pr) * 4 + (tau & 3)) * 1024 + lane * 16;
        GST16WT(dst, o);
      }
    };
    utile(1);
    up_epi(0);
    utile(2);
    utile(3);
    up_epi(1);
  } else {
    ssm_epi(acc1[0], colsum(0, 0), 0);
    ssm_epi(acc1[1], colsum(0, 1), 1);
    utile(1);
    ssm_epi(acc1[2], colsum(0, 2), 2);
    ssm_epi(acc1[3], colsum(0, 3), 3);
    utile(2);
    ssm_epi(acc2[0], colsum(1, 0), 4);
    ssm_epi(acc2[1], colsum(1, 1), 5);
    utile(3);
    ssm_epi(acc2[2], colsum(1, 2), 6);
    ssm_epi(acc2[3], colsum(1, 3), 7);
  }
  STAMP(13);
  BARRIER();
#pragma unroll
  for (int rd = 0; rd < (GT ? 1 : 2); ++rd) {
    if (GT ? (w == 0) : (rd == 0 || w == 0)) {
      const int ti = GT ? 0 : (rd == 0 ? w : 8);
      const f32x4 a8 = *(LAS f32x4*)(lds + L1_PW + (ti * 64 + q * 16 + 7) * 16);
      f32x4 h = *(LAS f32x4*)(lds + L1_AGG + ti * 1024 + lane * 16);
      const f32x4 a16 = cmul(a8, a8), a32 = cmul(a16, a16), a64 = cmul(a32, a32), a128 = cmul(a64, a64);
      h = cfma(a16, dpp4<0x111>(h), h);
      h = cfma(a32, dpp4<0x112>(h), h);
      h = cfma(a64, dpp4<0x114>(h), h);
      h = cfma(a128, dpp4<0x118>(h), h);
      const f32x4 ex = dpp4<0x111>(h);
      const int pair = (sig0 + ti) * 4 + q;
      *(f32x4*)(ws + OFF_CAR + ((size_t)(pi * 16 + t16) * 256 + pair) * 16) = ex;
      if (t16 == 15) *(f32x4*)(ws + OFF_TOT + ((size_t)pi * 256 + pair) * 16) = h;
      if (pi == 0 && t16 == 0) {
        const f32x4 a = *(LAS f32x4*)(lds + L1_AC + (ti * 4 + q) * 16);
        *(f32x4*)(ws + OFF_CT + (size_t)pair * 16) = a;
        *(f32x4*)(ws + OFF_CT + (size_t)(256 + pair) * 16) = a16;
        *(f32x4*)(ws + OFF_CT + (size_t)(512 + pair) * 16) = cmul(a128, a128);
      }
    }
  }
  if (blk == 0 && tid < 32) *(unsigned*)(ws + OFF_FL + tid * 128) = 0u;
  STAMP(14);
}

__global__ __launch_bounds__(512, 2) void k1(K1Args A) {
  extern __shared__ __attribute__((aligned(16))) char smem[];
  const int b = blockIdx.x;
  const int xcd = b & 7, r = b >> 3;
  const int pi = (xcd & 1) * 4 + (r & 3), nb = (xcd >> 1) * 8 + (r >> 2);
  const int c0 = ((r >> 2) + 2 * (r & 3)) & 7;
  if (nb < 28) k1_body<true>(A, smem, pi, nb, c0);
  else k1_body<false>(A, smem, pi, nb, c0);
}

struct K2Args {
  const u16 *x, *Dk, *wd, *Cre, *Cim;
  char* ws;
  float* out;
};
constexpr int L2_CAR = 131072;
constexpr int L2_EX = L2_CAR + 2048;
constexpr int L2_SIZE = L2_EX + 16384;

__global__ __launch_bounds__(512, 2) void k2(K2Args A) {
  extern __shared__ __attribute__((aligned(16))) char smem[];
  LAS char* lds = (LAS char*)smem;
  const unsigned lds0 = (unsigned)(uintptr_t)lds;
  const int tid = threadIdx.x, lane = tid & 63;
  const int w = __builtin_amdgcn_readfirstlane(tid >> 6);
  const int t16 = lane & 15, q = lane >> 4;
  const int b = blockIdx.x;
  const int xcd = b & 7, r = b >> 3;
  const int mi = xcd * 4 + (r >> 3), nj = r & 7;
  char* ws = A.ws;
  STAMP(0);
  const f32x4 zero4 = {0.f, 0.f, 0.f, 0.f};
  const int r8 = lane >> 3, pc = lane & 7;

  f32x4 ctot[7], ccar, ca16, ca256;
  const int cj = tid >> 5, cp = tid & 31;
  const int pi = mi >> 2;
  if (w < 2) {
    const char* tp = ws + OFF_TOT + (size_t)(32 * nj + cp) * 16;
#pragma unroll
    for (int i = 0; i < 7; ++i) { const char* p = tp + (size_t)(i < pi ? i : 0) * 4096; GLD16(ctot[i], p); }
    { const char* p = ws + OFF_CAR + ((size_t)(4 * mi + cj) * 256 + 32 * nj + cp) * 16; GLD16(ccar, p); }
    { const char* p = ws + OFF_CT + (size_t)(256 + 32 * nj + cp) * 16; GLD16(ca16, p); }
    { const char* p = ws + OFF_CT + (size_t)(512 + 32 * nj + cp) * 16; GLD16(ca256, p); }
  }
  const int tl = w >> 1, gl = w & 1;
  f32x4 vv[4], aa[4];
#pragma unroll
  for (int s = 0; s < 4; ++s) {
    const char* p = ws + OFF_V + (size_t)((4 * mi + tl) * 64 + 8 * nj + 4 * gl + s) * 1024 + lane * 16;
    GLD16(vv[s], p);
  }
#pragma unroll
  for (int s = 0; s < 4; ++s) {
    const char* p = ws + OFF_CT + (size_t)((8 * nj + 4 * gl + s) * 4 + q) * 16;
    GLD16(aa[s], p);
  }
  const char* asrc = ws + OFF_A + (size_t)mi * KS_TOT * 4096 + w * 2048 + lane * 16;
  const u16* wsrc = A.wd + (size_t)(16 * w + r8) * D_ + 64 * nj + 8 * (pc ^ (((r8 >> 1) & 3) << 1));
  const size_t coff = (size_t)(64 * nj + 8 * w + r8) * 512 + 8 * (pc ^ ((4 * (w & 1) + (r8 >> 1)) & 7));
  auto issue = [&](int s) {
    const unsigned sl = lds0 + (s & 3) * 32768;
    if (s < 14) {
      glds16(asrc + (size_t)s * 16384, sl + w * 2048);
      glds16(asrc + (size_t)s * 16384 + 1024, sl + w * 2048 + 1024);
    } else {
      glds16c(asrc + (size_t)s * 16384, sl + w * 2048);
      glds16c(asrc + (size_t)s * 16384 + 1024, sl + w * 2048 + 1024);
    }
    if (s < 14) {
      glds16(wsrc + (size_t)s * 128 * D_, sl + 16384 + w * 2048);
      glds16(wsrc + (size_t)s * 128 * D_ + 8 * D_, sl + 16384 + w * 2048 + 1024);
    } else {
      glds16(A.Cre + coff + (s - 14) * 64, sl + 16384 + w * 1024);
      glds16(A.Cim + coff + (s - 14) * 64, sl + 24576 + w * 1024);
    }
  };
  issue(0); issue(1); issue(2);
  STAMP(1);
  if (w < 2) {
    asm volatile("s_waitcnt vmcnt(20)"
                 : "+v"(ctot[0]), "+v"(ctot[1]), "+v"(ctot[2]), "+v"(ctot[3]), "+v"(ctot[4]), "+v"(ctot[5]), "+v"(ctot[6]),
                   "+v"(ccar), "+v"(ca16), "+v"(ca256)::"memory");
    f32x4 cpn = zero4;
#pragma unroll
    for (int i = 0; i < 7; ++i) {
      const f32x4 nx = cfma(cpn, ca256, ctot[i]);
      cpn = (i < pi) ? nx : cpn;
    }
    const int jl = (4 * mi + cj) & 15;
    const f32x4 one = {1.f, 1.f, 0.f, 0.f};
    f32x4 p = (jl & 1) ? ca16 : one;
    f32x4 s = cmul(ca16, ca16);
    p = cmul(p, (jl & 2) ? s : one);
    s = cmul(s, s);
    p = cmul(p, (jl & 4) ? s : one);
    s = cmul(s, s);
    p = cmul(p, (jl & 8) ? s : one);
    *(LAS f32x4*)(lds + L2_CAR + tid * 16) = cfma(p, cpn, ccar);
  }
  BARRIER();
  STAMP(2);
  asm volatile("s_waitcnt vmcnt(12)"
               : "+v"(vv[0]), "+v"(vv[1]), "+v"(vv[2]), "+v"(vv[3]), "+v"(aa[0]), "+v"(aa[1]), "+v"(aa[2]), "+v"(aa[3])::"memory");
  {
    u32x4 ore, oim;
#pragma unroll
    for (int s = 0; s < 4; ++s) {
      const f32x4 c = *(LAS f32x4*)(lds + L2_CAR + (tl * 32 + (4 * gl + s) * 4 + q) * 16);
      const f32x4 a = aa[s], a2 = cmul(a, a), a4 = cmul(a2, a2), a8 = cmul(a4, a4);
      f32x4 h = vv[s];
      h = cfma(a, (t16 == 0) ? c : zero4, h);
      h = cfma(a, dpp4<0x111>(h), h);
      h = cfma(a2, dpp4<0x112>(h), h);
      h = cfma(a4, dpp4<0x114>(h), h);
      h = cfma(a8, dpp4<0x118>(h), h);
      ore[s] = pk2(h[0], h[1]);
      oim[s] = pk2(-h[2], -h[3]);
    }
    char* dst = ws + OFF_A + ((size_t)(mi * KS_TOT + 56 + 4 * nj + 2 * gl) * 4 + tl) * 1024 + lane * 16;
    GST16WT(dst, ore);
    char* dst2 = dst + 4096;
    GST16WT(dst2, oim);
  }
  STAMP(3);

  const int kg = w >> 2, wr = w & 1, wc = (w >> 1) & 1;
  const int qq = t16 >> 2, pp = t16 & 3;
  const int trb = (4 * q + qq) * 128 + 32 * (((2 * q + (qq >> 1)) & 3) ^ (2 * wc)) + 8 * pp + kg * 4096;
  const int crb = (32 * wc + t16) * 128 + 16 * (q ^ ((t16 >> 1) & 7)) + kg * 8192;
  const int arb = (kg * 4 + 2 * wr) * 1024 + lane * 16;
  f32x4 accg[2][2], accy[2][2];
#pragma unroll
  for (int i = 0; i < 2; ++i)
#pragma unroll
    for (int j = 0; j < 2; ++j) { accg[i][j] = zero4; accy[i][j] = zero4; }
  unsigned* flag = (unsigned*)(ws + OFF_FL + mi * 128);
  unsigned pollv = 0;
  u32x2 xv[2], dv[2];
  f32x2 st;
  f32x4 sgv[2];
  const int orow = 64 * mi + 16 * (2 * wr + kg) + t16;
  const int ocol = 64 * nj + 32 * wc + 4 * q;
#pragma unroll
  for (int s = 0; s < 22; ++s) {
    constexpr int NW[22] = {10, 10, 10, 8, 9, 9, 8, 8, 9, 9, 9, 8, 8, 8, 8, 8, 13, 13, 13, 8, 4, 0};
    if (s == 11) {
      asm volatile("s_waitcnt vmcnt(8)" : "+v"(pollv)::"memory");
      unsigned spins = 0;
      while (__builtin_amdgcn_readfirstlane(pollv) < 64u) {
        __builtin_amdgcn_s_sleep(1);
        asm volatile("global_load_dword %0, %1, off sc1\n\ts_waitcnt vmcnt(0)" : "=v"(pollv) : "v"(flag) : "memory");
        if (++spins > (1u << 22)) break;
      }
    } else if (s == 19) {
      asm volatile("s_waitcnt vmcnt(8)" : "+v"(xv[0]), "+v"(xv[1]), "+v"(dv[0]), "+v"(dv[1]), "+v"(st)::"memory");
    } else {
      VMCNT(NW[s]);
    }
    BARRIER();
    if (s == 3 && lane == 0) asm volatile("global_atomic_add %0, %1, off" ::"v"(flag), "v"(1u) : "memory");
    if (s + 3 < 22) issue(s + 3);
    if (s == 7) asm volatile("global_load_dword %0, %1, off sc1" : "=v"(pollv) : "v"(flag) : "memory");
    if (s == 15) {
      const u16* xp = A.x + (size_t)orow * D_ + ocol;
      const u16* xp2 = xp + 16;
      const u16* dp = A.Dk + ocol;
      const u16* dp2 = dp + 16;
      const char* sp = ws + OFF_ST + (size_t)orow * 8;
      GLD8(xv[0], xp); GLD8(xv[1], xp2); GLD8(dv[0], dp); GLD8(dv[1], dp2); GLD8(st, sp);
    }
    LAS char* sl = lds + (s & 3) * 32768;
    if (s == 14) {
#pragma unroll
      for (int e = 0; e < 2; ++e) {
        const f32x4 g4 = accg[e][kg] + *(LAS f32x4*)(lds + L2_EX + ((w ^ 4) * 2 + e) * 1024 + lane * 16);
#pragma unroll
        for (int rr = 0; rr < 4; ++rr) {
          const float g = rbf(g4[rr]);
          sgv[e][rr] = rbf(g / (1.f + __expf(-g)));
        }
      }
    }
#pragma unroll
    for (int j = 0; j < 2; ++j) {
      bf16x8 af[2], bf[2];
#pragma unroll
      for (int e = 0; e < 2; ++e) af[e] = *(LAS bf16x8*)(sl + arb + j * 8192 + e * 1024);
#pragma unroll
      for (int e = 0; e < 2; ++e) {
        if (s < 14) {
          LAS char* p = sl + 16384 + j * 8192 + (trb ^ (e << 5));
          s16x4 lo = __builtin_amdgcn_ds_read_tr16_b64_v4i16((LAS s16x4*)p);
          s16x4 hi = __builtin_amdgcn_ds_read_tr16_b64_v4i16((LAS s16x4*)(p + 2048));
          bf[e] = __builtin_shufflevector(lo, hi, 0, 1, 2, 3, 4, 5, 6, 7);
        } else {
          bf[e] = *(LAS bf16x8*)(sl + 16384 + e * 2048 + (crb ^ (j << 6)));
        }
      }
#pragma unroll
      for (int e = 0; e < 2; ++e)
#pragma unroll
        for (int tt = 0; tt < 2; ++tt) {
          if (s < 14) accg[e][tt] = MFMA(bf[e], af[tt], accg[e][tt]);
          else accy[e][tt] = MFMA(bf[e], af[tt], accy[e][tt]);
        }
    }
    if (s == 13) {
#pragma unroll
      for (int e = 0; e < 2; ++e) *(LAS f32x4*)(lds + L2_EX + (w * 2 + e) * 1024 + lane * 16) = kg ? accg[e][0] : accg[e][1];
    }
  }
#pragma unroll
  for (int e = 0; e < 2; ++e) *(LAS f32x4*)(lds + L2_EX + (w * 2 + e) * 1024 + lane * 16) = kg ? accy[e][0] : accy[e][1];
  BARRIER();
#pragma unroll
  for (int e = 0; e < 2; ++e) {
    const f32x4 y4 = (kg ? accy[e][1] : accy[e][0]) + *(LAS f32x4*)(lds + L2_EX + ((w ^ 4) * 2 + e) * 1024 + lane * 16);
    f32x4 o;
#pragma unroll
    for (int rr = 0; rr < 4; ++rr) {
      const float xx = bf2f((xv[e][rr >> 1] >> (16 * (rr & 1))) & 0xffff);
      const float dd = bf2f((dv[e][rr >> 1] >> (16 * (rr & 1))) & 0xffff);
      const float xn = rbf(rbf(xx - st[0]) / st[1]);
      const float y = 2.f * y4[rr] + rbf(xn * dd);
      o[rr] = xx + y * sgv[e][rr];
    }
    *(f32x4*)(A.out + (size_t)orow * D_ + ocol + 16 * e) = o;
  }
  STAMP(11);
}

extern "C" void kernel_launch(void* const* d_in, const int* in_sizes, int n_in, void* d_out, int out_size, void* d_ws,
                              size_t ws_size, hipStream_t stream) {
  K1Args a;
  a.x = (const u16*)d_in[0]; a.Lre = (const u16*)d_in[1]; a.Lim = (const u16*)d_in[2];
  a.Bre = (const u16*)d_in[3]; a.Bim = (const u16*)d_in[4]; a.Cre = (const u16*)d_in[5]; a.Cim = (const u16*)d_in[6];
  a.ldt = (const u16*)d_in[7]; a.wg = (const u16*)d_in[10]; a.wu = (const u16*)d_in[11]; a.wd = (const u16*)d_in[12];
  a.ws = (char*)d_ws;
  (void)hipFuncSetAttribute((const void*)k1, hipFuncAttributeMaxDynamicSharedMemorySize, L1_SIZE);
  (void)hipFuncSetAttribute((const void*)k2, hipFuncAttributeMaxDynamicSharedMemorySize, L2_SIZE);
  k1<<<256, 512, L1_SIZE, stream>>>(a);
  K2Args b;
  b.x = (const u16*)d_in[0]; b.Dk = (const u16*)d_in[8]; b.wd = (const u16*)d_in[12];
  b.Cre = (const u16*)d_in[5]; b.Cim = (const u16*)d_in[6]; b.ws = (char*)d_ws; b.out = (float*)d_out;
  k2<<<256, 512, L2_SIZE, stream>>>(b);
}
```

```cpp
#include <hip/hip_runtime.h>
#include <hip/hip_bf16.h>
#include <stdint.h>

typedef unsigned short u16;
typedef __attribute__((ext_vector_type(8))) short bf16x8;
typedef __attribute__((ext_vector_type(4))) short s16x4;
typedef __attribute__((ext_vector_type(4))) float f32x4;
typedef __attribute__((ext_vector_type(2))) float f32x2;
typedef __attribute__((ext_vector_type(4))) unsigned u32x4;
typedef __attribute__((ext_vector_type(2))) unsigned u32x2;
typedef __attribute__((ext_vector_type(2))) __bf16 bf16x2_t;
#define LAS __attribute__((address_space(3)))
#if defined(__HIP_DEVICE_COMPILE__)
#define NOPK __attribute__((target("no-packed-fp32-ops")))
#else
#define NOPK
#endif

constexpr int D_ = 512, F_ = 1792;

constexpr size_t OFF_A   = 0;
constexpr size_t OFF_H   = OFF_A + (size_t)32 * 56 * 4096;
constexpr size_t OFF_V   = OFF_H + (size_t)32 * 8 * 16384;
constexpr size_t OFF_CAR = OFF_V + (size_t)64 * 64 * 2048;
constexpr size_t OFF_TOT = OFF_CAR + (size_t)16 * 64 * 1024;
constexpr size_t OFF_ST  = OFF_TOT + (size_t)16 * 64 * 64;
constexpr size_t OFF_FL  = OFF_ST + (size_t)2048 * 8;

constexpr int L1_STP = 131072;
constexpr int L1_STF = L1_STP + 2048;
constexpr int L1_CS  = L1_STF + 2048;
constexpr int L1_AC  = L1_CS + 2560;
constexpr int L1_AGG = L1_AC + 2304;
constexpr int L1_DUM = L1_AGG + 18432;
constexpr int L1_SIZE = L1_DUM + 1024;

__device__ __forceinline__ float bf2f(unsigned h) { return __builtin_bit_cast(float, h << 16); }
__device__ __forceinline__ float rbf(float v) { return (float)(__bf16)v; }
__device__ __forceinline__ unsigned pk2(float lo, float hi) {
  bf16x2_t r = {(__bf16)lo, (__bf16)hi};
  return __builtin_bit_cast(unsigned, r);
}
template <int CTRL> __device__ __forceinline__ float dpp0(float x) {
  return __builtin_bit_cast(float, __builtin_amdgcn_update_dpp(0, __builtin_bit_cast(int, x), CTRL, 0xf, 0xf, true));
}
template <int CTRL> __device__ __forceinline__ f32x4 dpp4(f32x4 x) {
  f32x4 r;
#pragma unroll
  for (int i = 0; i < 4; ++i) r[i] = dpp0<CTRL>(x[i]);
  return r;
}
#define SWP(x) dpp0<0xB1>(x)
__device__ __forceinline__ f32x4 csq2(f32x4 a) {
  f32x4 r;
  r[0] = fmaf(a[0], a[0], -a[1] * a[1]); r[1] = 2.f * a[0] * a[1];
  r[2] = fmaf(a[2], a[2], -a[3] * a[3]); r[3] = 2.f * a[2] * a[3];
  return r;
}
__device__ __forceinline__ f32x4 cfma2(f32x4 a, f32x4 b, f32x4 c) {
  f32x4 r;
  r[0] = fmaf(a[0], b[0], fmaf(-a[1], b[1], c[0]));
  r[1] = fmaf(a[0], b[1], fmaf(a[1], b[0], c[1]));
  r[2] = fmaf(a[2], b[2], fmaf(-a[3], b[3], c[2]));
  r[3] = fmaf(a[2], b[3], fmaf(a[3], b[2], c[3]));
  return r;
}
__device__ __forceinline__ f32x2 csq(f32x2 a) { return (f32x2){fmaf(a[0], a[0], -a[1] * a[1]), 2.f * a[0] * a[1]}; }
__device__ __forceinline__ f32x2 cmul(f32x2 a, f32x2 b) {
  return (f32x2){fmaf(a[0], b[0], -a[1] * b[1]), fmaf(a[0], b[1], a[1] * b[0])};
}
template <int V> struct IC { static constexpr int value = V; };
template <int I, int N, class F> __device__ __forceinline__ void sfor(F&& f) {
  if constexpr (I < N) { f(IC<I>{}); sfor<I + 1, N>(f); }
}
__device__ __forceinline__ const char* uptr(const void* base, unsigned off) {
  return (const char*)base + (size_t)(unsigned)__builtin_amdgcn_readfirstlane(off);
}
#define MFMA(a, b, c) __builtin_amdgcn_mfma_f32_16x16x32_bf16(a, b, c, 0, 0, 0)
#define SCHED() __builtin_amdgcn_sched_barrier(0)

#define GLD16(dst, p) asm volatile("global_load_dwordx4 %0, %1, off" : "=v"(dst) : "v"(p) : "memory")
#define GLD8(dst, p) asm volatile("global_load_dwordx2 %0, %1, off" : "=v"(dst) : "v"(p) : "memory")
#define GLD4(dst, p) asm volatile("global_load_dword %0, %1, off" : "=v"(dst) : "v"(p) : "memory")
#define GLD2(dst, p) asm volatile("global_load_ushort %0, %1, off" : "=v"(dst) : "v"(p) : "memory")
#define GST16S(sb, vo, v) asm volatile("global_store_dwordx4 %0, %1, %2 sc0 sc1\n\ts_nop 1" ::"v"(vo), "v"(v), "s"(sb) : "memory")
#define VMCNT(n) asm volatile("s_waitcnt vmcnt(%0)" ::"i"(n) : "memory")
#define BARRIER() do { asm volatile("s_waitcnt lgkmcnt(0)" ::: "memory"); __builtin_amdgcn_s_barrier(); asm volatile("" ::: "memory"); } while (0)
__device__ __forceinline__ void dma(const char* sbase, unsigned voff, unsigned lds_dst) {
  asm volatile("s_mov_b32 m0, %2\n\ts_nop 0\n\tglobal_load_lds_dwordx4 %0, %1" ::"v"(voff), "s"(sbase), "s"(lds_dst) : "memory");
}
__device__ __forceinline__ void dmac(const char* sbase, unsigned voff, unsigned lds_dst) {
  asm volatile("s_mov_b32 m0, %2\n\ts_nop 0\n\tglobal_load_lds_dwordx4 %0, %1 sc1" ::"v"(voff), "s"(sbase), "s"(lds_dst) : "memory");
}

#define STAMP(i)
constexpr size_t OFF_DBG = (size_t)64 << 20;
#define RT0()
#define RT1(a,b,c)

__device__ __forceinline__ f32x2 ssm_a(unsigned plr, unsigned pli, unsigned pdt) {
  const float dt = rbf(expf(bf2f(pdt)));
  float s, k;
  sincosf(dt * bf2f(pli), &s, &k);
  const float e = expf(dt * bf2f(plr));
  return (f32x2){e * k, e * s};
}

struct K1Args {
  const u16 *x, *Lre, *Lim, *Bre, *Bim, *Cre, *Cim, *ldt, *wg, *wu, *wd;
  char* ws;
};

constexpr int k1_nring(int s) { return s > 14 ? 0 : (s < 8 ? 3 : 4); }
constexpr int k1_pf(int s) { return s < 8 ? 0 : (s < 12 ? 2 : (s == 12 ? 3 : 0)); }
constexpr int k1_st(bool gt, int s) {
  if (s < 8) return 0;
  if (gt) return s == 12 ? 2 : (s == 13 ? 2 : 0);
  return s == 13 ? 4 : (s == 14 ? 0 : 2);
}
constexpr int k1_wait(bool gt, int s) {
  int n = k1_nring(s + 1) + k1_nring(s + 2);
  for (int t = s - 3; t < s; ++t)
    if (t >= 0) n += k1_pf(t) + k1_st(gt, t);
  return n;
}
constexpr int k1_glo(int s) {
  return s <= 8 ? 0 : (s == 9 ? 5 : (s == 10 ? 11 : (s == 11 ? 17 : (s == 12 ? 23 : (s == 13 ? 26 : (s == 14 ? 30 : 32))))));
}

template <bool GT>
__device__ __forceinline__ void k1_body(const K1Args& A, char* smem, int pi, int nb, int c0, int rx) {
  const int tid = threadIdx.x, lane = tid & 63;
  const int w = __builtin_amdgcn_readfirstlane(tid >> 6);
  const int rg = w & 3, cg = w >> 2;
  const int t16 = lane & 15, q = lane >> 4;
  const int blk = pi * 16 + nb;
  STAMP(0);
  RT0();
  LAS char* lds = (LAS char*)smem;
  const unsigned lds0 = (unsigned)(uintptr_t)lds;
  const int r8 = lane >> 3, pc = lane & 7;
  const int sgb = GT ? 2 * nb - 8 : 28 + 18 * (nb - 14);
  const int sgs = GT ? 1 : 9;

  const char* xb = (const char*)A.x + (size_t)pi * 131072;
  const unsigned vx0 = (16 * w + r8) * 1024 + 16 * (pc ^ (((r8 >> 1) & 1) | (((2 * w) & 3) << 1)));
  const unsigned vx1 = (16 * w + 8 + r8) * 1024 + 16 * (pc ^ (((r8 >> 1) & 1) | (((2 * w + 1) & 3) << 1)));
  const unsigned vs = r8 * 1024 + 16 * (pc ^ ((w & 1) | ((r8 >> 1) << 1)));
  const unsigned vw = GT ? (8 * w + r8) * 3584 + 16 * (pc ^ ((((r8 >> 1) & 1) | ((w & 1) << 1)) << 1)) : vs;
  const char* bmat = (const char*)((w & 1) ? A.Bim : A.Bre);
  auto wbase = [&](int p, int c) -> const char* {
    if (GT) return (const char*)(p < 2 ? A.wg : A.wu) + (size_t)c * (64 * 3584) + (128 * nb + 64 * (p & 1)) * 2;
    return uptr(bmat, (sgb + sgs * cg + 2 * p + ((w >> 1) & 1)) * 8192 + c * 128);
  };
  auto issue = [&](int s) {
    const unsigned dst = lds0 + (s & 3) * 32768;
    if (s < 8) {
      const int c = (c0 + s) & 7;
      dma(xb + c * 128, vx0, dst + w * 2048);
      dma(wbase(0, c), vw, dst + 16384 + w * 1024);
      dma(xb + c * 128, vx1, dst + w * 2048 + 1024);
    } else if (s == 12) {
      const char* sb = uptr(bmat, (sgb + sgs * ((w >> 1) & 1) + 8) * 8192);
#pragma unroll
      for (int d = 0; d < 4; ++d) {
        const int i = 4 * cg + d;
        dma(sb + ((c0 + i) & 7) * 128, vs, dst + i * 4096 + rg * 1024);
      }
    } else {
      const int p = s < 12 ? (s - 6) >> 1 : 3, half = s < 12 ? (s & 1) : (s - 13);
#pragma unroll
      for (int d = 0; d < 4; ++d) dma(wbase(p, (c0 + 4 * half + d) & 7), vw, dst + d * 8192 + w * 1024);
    }
  };
  auto prefetch = [&](int n) {
    const char* sb = n < 7 ? (const char*)A.wd + (size_t)rx * 57344 + (n * 8 + w) * 1024
                           : (const char*)(n < 9 ? A.Cre : A.Cim) + (size_t)rx * 16384 + (((n - 7) & 1) * 8 + w) * 1024;
    int l = lane;
    asm volatile("" : "+v"(l));
    dma(sb, l * 16, lds0 + L1_DUM);
  };

  unsigned plr, pli, pdt;
  const int tlp = GT ? 8 : (4 * rg + q < 8 ? 4 * rg + q : 8);
  {
    const int ch = 8 * (sgb + sgs * cg + tlp) + (t16 >> 1);
    GLD2(plr, A.Lre + ch); GLD2(pli, A.Lim + ch); GLD2(pdt, A.ldt + ch);
  }
  issue(0);
  __builtin_amdgcn_s_barrier();
  issue(1);
  __builtin_amdgcn_s_barrier();
  issue(2);
  STAMP(1);
  {
    asm volatile("s_waitcnt vmcnt(9)" : "+v"(plr), "+v"(pli), "+v"(pdt)::"memory");
    f32x2 a = ssm_a(plr, pli, pdt);
    if (!(t16 & 1)) a[1] = -a[1];
    *(LAS f32x2*)(lds + L1_AC + ((cg * 9 + tlp) * 16 + t16) * 8) = a;
  }
  STAMP(2);

  const f32x4 zero4 = {0.f, 0.f, 0.f, 0.f};
  const unsigned ONES = 0x3F803F80u;
  const bf16x8 onesf = __builtin_bit_cast(bf16x8, (u32x4){ONES, ONES, ONES, ONES});
  const int qq = t16 >> 2, pp = t16 & 3;
  const int tb = (8 * q + qq) * 128 + 32 * ((qq >> 1) | ((q & 1) << 1)) + 8 * pp;
  const int sx = (pp >> 1) | (qq << 1);
  const int ax0 = (32 * rg + 8 * qq + pp) * 128 + 16 * ((4 * cg + q) ^ sx);
  const int ss = (t16 & 1) | (qq << 1);
  const int sr = (8 * (t16 & 1) + (t16 >> 1)) * 128;
  const int as0 = cg * 2048 + sr + 16 * ((4 * cg + q) ^ ss);
  auto olane = [&]() -> int { int l = lane; asm volatile("" : "+v"(l)); return l; };
  int aw[2][2] = {
      {GT ? (tb ^ (cg << 6)) + cg * 4096 : as0 + cg * 2048, GT ? (tb ^ (cg << 6)) + (cg ^ 1) * 4096 : (as0 ^ 64) + cg * 2048},
      {GT ? (tb ^ (cg << 6) ^ 32) + cg * 4096 : as0 + cg * 2048 + 2048,
       GT ? (tb ^ (cg << 6) ^ 32) + (cg ^ 1) * 4096 : (as0 ^ 64) + cg * 2048 + 2048}};
  int awc = GT ? (tb ^ ((2 * cg + (rg & 1)) << 5)) + (rg >> 1) * 4096
                     : (2 * cg + (rg & 1)) * 2048 + sr + 16 * ((4 * (rg >> 1) + q) ^ ss);
  auto advance = [&](int s) {
    const int d = (s & 3) ? 32768 : -98304;
    aw[0][0] += d; aw[0][1] += d; awc += d;
    asm volatile("" : "+v"(aw[0][0]), "+v"(aw[0][1]), "+v"(awc));
    if (GT) {
      aw[1][0] += d; aw[1][1] += d;
      asm volatile("" : "+v"(aw[1][0]), "+v"(aw[1][1]));
    } else {
      aw[1][0] = aw[0][0] + 2048; aw[1][1] = aw[0][1] + 2048;
    }
  };
  auto wfrag = [&](int off) -> bf16x8 {
    if (GT) {
      LAS char* p = lds + off;
      s16x4 lo = __builtin_amdgcn_ds_read_tr16_b64_v4i16((LAS s16x4*)p);
      s16x4 hi = __builtin_amdgcn_ds_read_tr16_b64_v4i16((LAS s16x4*)(p + 512));
      return __builtin_shufflevector(lo, hi, 0, 1, 2, 3, 4, 5, 6, 7);
    } else {
      return *(LAS bf16x8*)(lds + off);
    }
  };
  auto mm = [&](bf16x8 wfv, bf16x8 xfv, f32x4 c) -> f32x4 { return GT ? MFMA(wfv, xfv, c) : MFMA(xfv, wfv, c); };

  f32x4 acc[4][2][2], accs[2], gr[2], sm[2], csa = zero4;
#pragma unroll
  for (int e = 0; e < 2; ++e) { acc[0][e][0] = zero4; acc[0][e][1] = zero4; gr[e] = zero4; sm[e] = zero4; }
  bf16x8 xf[2][16];
  bf16x8 wd0, wd1, cwd;
#pragma unroll
  for (int i = 0; i < 8; ++i) {
    VMCNT(k1_wait(GT, i));
    BARRIER();
    if (i < 8) STAMP(3 + i);
    const int sl = (i & 3) * 32768;
    xf[0][2 * i] = *(LAS bf16x8*)(lds + sl + ax0);
    xf[1][2 * i] = *(LAS bf16x8*)(lds + sl + 512 + ax0);
    const bf16x8 w0 = wfrag(16384 + aw[0][0]), w1 = wfrag(16384 + aw[1][0]);
    SCHED();
    issue(i + 3);
    if (i > 0) {
      acc[0][0][0] = mm(wd0, xf[0][2 * i - 1], acc[0][0][0]);
      acc[0][0][1] = mm(wd0, xf[1][2 * i - 1], acc[0][0][1]);
      acc[0][1][0] = mm(wd1, xf[0][2 * i - 1], acc[0][1][0]);
      acc[0][1][1] = mm(wd1, xf[1][2 * i - 1], acc[0][1][1]);
      csa = MFMA(cwd, onesf, csa);
    }
    SCHED();
    xf[0][2 * i + 1] = *(LAS bf16x8*)(lds + sl + (ax0 ^ 64));
    xf[1][2 * i + 1] = *(LAS bf16x8*)(lds + sl + 512 + (ax0 ^ 64));
    wd0 = wfrag(16384 + aw[0][1]);
    wd1 = wfrag(16384 + aw[1][1]);
    cwd = wfrag(16384 + awc);
    advance(i + 1);
    SCHED();
#pragma unroll
    for (int jt = 0; jt < 2; ++jt) {
      gr[jt] = MFMA(xf[jt][2 * i], xf[jt][2 * i], gr[jt]);
      sm[jt] = MFMA(onesf, xf[jt][2 * i], sm[jt]);
    }
    acc[0][0][0] = mm(w0, xf[0][2 * i], acc[0][0][0]);
    acc[0][0][1] = mm(w0, xf[1][2 * i], acc[0][0][1]);
    acc[0][1][0] = mm(w1, xf[0][2 * i], acc[0][1][0]);
    acc[0][1][1] = mm(w1, xf[1][2 * i], acc[0][1][1]);
    SCHED();
  }
  csa = MFMA(cwd, onesf, csa);
  acc[0][0][0] = mm(wd0, xf[0][15], acc[0][0][0]);
  acc[0][0][1] = mm(wd0, xf[1][15], acc[0][0][1]);
  acc[0][1][0] = mm(wd1, xf[0][15], acc[0][1][0]);
  acc[0][1][1] = mm(wd1, xf[1][15], acc[0][1][1]);
#pragma unroll
  for (int jt = 0; jt < 2; ++jt) {
    const float dg = pp == 0 ? gr[jt][0] : (pp == 1 ? gr[jt][1] : (pp == 2 ? gr[jt][2] : gr[jt][3]));
    if (q == qq) *(LAS f32x2*)(lds + L1_STP + (cg * 128 + rg * 32 + jt * 16 + t16) * 8) = (f32x2){dg, sm[jt][0]};
  }
  auto cs_put = [&](int pass) {
    const int l = olane();
    if ((l & 15) == 0) *(LAS f32x4*)(lds + L1_CS + ((cg * 5 + pass) * 4 + rg) * 64 + l) = csa;
  };
  cs_put(0);

  char* ws = A.ws;
  float nmb[2], sdv[2], rsg[2];
  f32x4 cgv;
  auto cs4 = [&](int pass, int e) -> f32x4 {
    const int o = L1_CS + ((cg * 5 + pass) * 4 + e) * 64 + (olane() & 48);
    const f32x4 a = *(LAS f32x4*)(lds + o), b = *(LAS f32x4*)(lds + o + 128);
    return (f32x4){a[0] + b[0], a[1] + b[1], a[2] + b[2], a[3] + b[3]};
  };
  auto stats = [&]() {
    const int l = olane(), t16 = l & 15, q = l >> 4, qq = t16 >> 2, pp = t16 & 3;
#pragma unroll
    for (int jt = 0; jt < 2; ++jt) {
      const f32x2 p0 = *(LAS f32x2*)(lds + L1_STP + (rg * 32 + jt * 16 + t16) * 8);
      const f32x2 p1 = *(LAS f32x2*)(lds + L1_STP + (128 + rg * 32 + jt * 16 + t16) * 8);
      const float mean = (p0[1] + p1[1]) * (1.f / 512.f);
      const float var = fmaxf((p0[0] + p1[0]) * (1.f / 512.f) - mean * mean, 0.f);
      const float mb = rbf(mean);
      const float sd = rbf(__builtin_sqrtf(rbf(rbf(var) + 1e-5f)));
      const float rs = 1.f / sd;
      nmb[jt] = -mb; sdv[jt] = sd; rsg[jt] = rs * -1.44269504088896f;
      if (q == 0) {
        *(LAS float*)(lds + L1_STF + (w * 64 + jt * 16 + t16) * 4) = -mb;
        *(LAS float*)(lds + L1_STF + (w * 64 + 32 + jt * 16 + t16) * 4) = rs;
        if (nb == 0 && cg == 0) *(f32x2*)(ws + OFF_ST + (size_t)(pi * 128 + rg * 32 + 8 * qq + 4 * jt + pp) * 8) = (f32x2){mb, sd};
      }
    }
  };
  auto gate_el = [&](int n) {
    const int p = n >> 4, e = (n >> 3) & 1, jt = (n >> 2) & 1, r = n & 3;
    if ((n & 7) == 0) cgv = cs4(p, e);
    const float t = fmaf(nmb[jt], cgv[r], acc[p][e][jt][r]) * rsg[jt];
    float sv = __builtin_amdgcn_rcpf(fmaf(__builtin_amdgcn_exp2f(t), sdv[jt], sdv[jt]));
    asm volatile("" : "+v"(sv));
    acc[p][e][jt][r] = sv;
  };
  auto up_epi = [&](int p, int jt, const f32x4& cu0, const f32x4& cu1) {
    u32x4 o;
#pragma unroll
    for (int e = 0; e < 2; ++e) {
      float hv[4];
#pragma unroll
      for (int r = 0; r < 4; ++r) hv[r] = fmaf(nmb[jt], e ? cu1[r] : cu0[r], acc[p][e][jt][r]) * acc[p - 2][e][jt][r];
      o[2 * e] = pk2(hv[0], hv[1]);
      o[2 * e + 1] = pk2(hv[2], hv[3]);
    }
    const char* sb = uptr(ws, (unsigned)OFF_A + (((2 * pi + (rg >> 1)) * 56 + 4 * nb + 2 * (p - 2) + cg) * 4 + 2 * (rg & 1) + jt) * 1024);
    const unsigned vl16 = olane() * 16;
    GST16S(sb, vl16, o);
  };
  float zrun;
  auto ssm_epi = [&](const f32x4& av, int jt, int pass, int e, int tl) {
    float cs;
    const int lane = olane(), t16 = lane & 15, q = lane >> 4;
    {
      const int o = L1_CS + (cg * 5 + pass) * 256 + t16 * 4;
      if (pass == 4) cs = (*(LAS float*)(lds + o) + *(LAS float*)(lds + o + 64)) + (*(LAS float*)(lds + o + 128) + *(LAS float*)(lds + o + 192));
      else cs = *(LAS float*)(lds + o + e * 64) + *(LAS float*)(lds + o + e * 64 + 128);
    }
    const f32x4 mq = *(LAS f32x4*)(lds + L1_STF + (w * 64 + jt * 16 + 4 * q) * 4);
    const f32x4 rq = *(LAS f32x4*)(lds + L1_STF + (w * 64 + 32 + jt * 16 + 4 * q) * 4);
    const f32x2 a = *(LAS f32x2*)(lds + L1_AC + ((cg * 9 + tl) * 16 + t16) * 8);
    f32x4 u;
#pragma unroll
    for (int r = 0; r < 4; ++r) u[r] = fmaf(mq[r], cs, av[r]) * rq[r];
    const char* sb = uptr(ws, (unsigned)OFF_V + (((4 * pi + rg) * 64 + sgb + sgs * cg + tl) * 2 + jt) * 1024);
    const unsigned vl16 = lane * 16;
    GST16S(sb, vl16, u);
    float z = jt ? fmaf(a[0], zrun, fmaf(a[1], SWP(zrun), u[0])) : u[0];
#pragma unroll
    for (int r = 1; r < 4; ++r) z = fmaf(a[0], z, fmaf(a[1], SWP(z), u[r]));
    zrun = z;
    if (jt) *(LAS float*)(lds + L1_AGG + (cg * 9 + tl) * 1024 + rg * 256 + lane * 4) = z;
  };
  auto prefix = [&](int g, int tl) {
    const int ti = g * 9 + tl;
    const int l = olane(), t16 = l & 15, q = l >> 4;
    const f32x2 a0 = *(LAS f32x2*)(lds + L1_AC + (ti * 16 + 4 * q + 1) * 8), a1 = *(LAS f32x2*)(lds + L1_AC + (ti * 16 + 4 * q + 3) * 8);
    f32x4 h = *(LAS f32x4*)(lds + L1_AGG + ti * 1024 + t16 * 64 + q * 16);
    f32x4 m = csq2(csq2(csq2((f32x4){a0[0], a0[1], a1[0], a1[1]})));
    h = cfma2(m, dpp4<0x111>(h), h); m = csq2(m);
    h = cfma2(m, dpp4<0x112>(h), h); m = csq2(m);
    h = cfma2(m, dpp4<0x114>(h), h); m = csq2(m);
    h = cfma2(m, dpp4<0x118>(h), h);
    const f32x4 ex = dpp4<0x111>(h);
    const int sig = sgb + sgs * g + tl;
    *(f32x4*)(ws + OFF_CAR + (size_t)(pi * 64 + sig) * 1024 + t16 * 64 + q * 16) = ex;
    if (t16 == 15) *(f32x4*)(ws + OFF_TOT + (size_t)(pi * 64 + sig) * 64 + q * 16) = h;
  };
  f32x4 cu0, cu1;
  auto quantum = [&](int s, int g) {
    if (GT) {
      const int off = (s == 8) ? 2 : ((s == 12 || s == 13) ? 4 : 0);
      if (s == 8 && g == 0) stats();
      if (s == 12 && g == 0) { cu0 = cs4(2, 0); cu1 = cs4(2, 1); }
      if (s == 12 && g == 1) up_epi(2, 0, cu0, cu1);
      if (s == 12 && g == 3) up_epi(2, 1, cu0, cu1);
      if (s == 13 && g == 0) ssm_epi(accs[0], 0, 4, 0, 8);
      if (s == 13 && g == 2) ssm_epi(accs[1], 1, 4, 0, 8);
      if (g >= off && k1_glo(s) + g - off < k1_glo(s + 1)) gate_el(k1_glo(s) + g - off);
      if (s == 14 && g == 3 && rg == 0) prefix(cg, 8);
    } else {
      if (s == 8 && g == 0) stats();
      if (s <= 13 && (g == 2 || g == 4)) {
        const int tl = s - 8;
        ssm_epi(acc[tl >> 1][tl & 1][g == 4], g == 4, tl >> 1, tl & 1, tl);
      }
      if (s == 13 && (g == 5 || g == 7)) ssm_epi(accs[g == 7], g == 7, 4, 0, 8);
      if (s == 12 && g == 6) prefix(cg, rg);
      if (s == 14 && g == 3 && rg < 3) prefix(cg, rg == 2 ? 8 : 4 + rg);
    }
  };
#pragma unroll
  for (int s = 8; s < 15; ++s) {
    int bs0, bsc;
    if (s == 12) {
      const int l = olane(), t = l & 15, qv = l >> 4;
      const int ss2 = (t & 1) | ((t >> 2) << 1), sr2 = (8 * (t & 1) + (t >> 1)) * 128;
      bs0 = cg * 2048 + sr2 + 16 * ((4 * cg + qv) ^ ss2);
      bsc = cg * 2048 + (rg & 1) * 4096 + sr2 + 16 * ((4 * (rg >> 1) + qv) ^ ss2);
    }
    VMCNT(k1_wait(GT, s));
    BARRIER();
    if (s == 8) STAMP(11);
    if (s == 12) STAMP(12);
    const int sl = (s & 3) * 32768;
    if (s == 12) {
      bf16x8 f[6], cw;
#pragma unroll
      for (int k = 0; k < 4; ++k) f[k] = *(LAS bf16x8*)(lds + sl + (k >> 1) * 4096 + (bs0 ^ ((k & 1) << 6)));
      SCHED();
      prefetch(8); prefetch(9); prefetch(10);
      accs[0] = zero4; accs[1] = zero4; csa = zero4;
#pragma unroll
      for (int g = 0; g < 8; ++g) {
        quantum(s, g);
        SCHED();
        if (g < 6) {
          f[(2 * g + 4) % 6] = *(LAS bf16x8*)(lds + sl + (g + 2) * 4096 + bs0);
          f[(2 * g + 5) % 6] = *(LAS bf16x8*)(lds + sl + (g + 2) * 4096 + (bs0 ^ 64));
        }
        if (!(g & 1)) cw = *(LAS bf16x8*)(lds + sl + (g >> 1) * 8192 + bsc);
        if (g == 7) advance(s + 1);
#pragma unroll
        for (int j = 0; j < 2; ++j) {
          accs[0] = MFMA(xf[0][2 * g + j], f[(2 * g + j) % 6], accs[0]);
          accs[1] = MFMA(xf[1][2 * g + j], f[(2 * g + j) % 6], accs[1]);
        }
        if (g & 1) csa = MFMA(cw, onesf, csa);
        SCHED();
      }
      cs_put(4);
    } else {
      const int p = s < 12 ? (s - 6) >> 1 : 3, half = s < 12 ? (s & 1) : (s - 13);
      bf16x8 f[3][2], cw[4];
#pragma unroll
      for (int g = 0; g < 2; ++g) { f[g][0] = wfrag(aw[0][g]); f[g][1] = wfrag(aw[1][g]); }
      if (s == 14) {
#pragma unroll
        for (int d = 0; d < 4; ++d) cw[d] = wfrag(d * 8192 + awc);
      }
      SCHED();
      if (s + 3 < 15) issue(s + 3);
      if (s < 12) { prefetch(2 * (s - 8)); prefetch(2 * (s - 8) + 1); }
      if (half == 0) {
#pragma unroll
        for (int e = 0; e < 2; ++e) { acc[p][e][0] = zero4; acc[p][e][1] = zero4; }
        csa = zero4;
      }
#pragma unroll
      for (int g = 0; g < 8; ++g) {
        quantum(s, g);
        SCHED();
        if (s == 14 && g == 1) {
#pragma unroll
          for (int d = 0; d < 4; ++d) csa = MFMA(cw[d], onesf, csa);
        }
        if (s == 14 && g == 3) cs_put(3);
        if (g < 6) {
          f[(g + 2) % 3][0] = wfrag(((g + 2) >> 1) * 8192 + aw[0][g & 1]);
          f[(g + 2) % 3][1] = wfrag(((g + 2) >> 1) * 8192 + aw[1][g & 1]);
        }
        if (s != 14 && !(g & 1)) cw[0] = wfrag((g >> 1) * 8192 + awc);
        if (s != 14 && g == 7) advance(s + 1);
        const int ks = 8 * half + g;
#pragma unroll
        for (int e = 0; e < 2; ++e) {
          acc[p][e][0] = mm(f[g % 3][e], xf[0][ks], acc[p][e][0]);
          acc[p][e][1] = mm(f[g % 3][e], xf[1][ks], acc[p][e][1]);
        }
        if (s != 14 && (g & 1)) csa = MFMA(cw[0], onesf, csa);
        SCHED();
        if (s == 14 && g == 5) {
          BARRIER();
          if (GT) { cu0 = cs4(3, 0); cu1 = cs4(3, 1); }
        }
      }
      if (half == 1 && s != 14) cs_put(p);
    }
  }
  STAMP(13);
  if (GT) {
    up_epi(3, 0, cu0, cu1);
    up_epi(3, 1, cu0, cu1);
  } else {
    ssm_epi(acc[3][0][0], 0, 3, 0, 6);
    ssm_epi(acc[3][0][1], 1, 3, 0, 6);
    ssm_epi(acc[3][1][0], 0, 3, 1, 7);
    ssm_epi(acc[3][1][1], 1, 3, 1, 7);
    BARRIER();
    if (rg < 2) prefix(cg, 6 + rg);
  }
  if (blk == 0 && tid < 32) *(unsigned*)(ws + OFF_FL + tid * 128) = 0u;
  STAMP(14);
  VMCNT(0);
  RT1(0, blk * 8 + w, ws);
}

__global__ __launch_bounds__(512, 2) NOPK void k1(K1Args A) {
  extern __shared__ __attribute__((aligned(16))) char smem[];
  const int b = blockIdx.x;
  const int xcd = b & 7, r = b >> 3;
  const int pl = r & 7, nl = r >> 3;
  const int pi = (xcd & 1) * 8 + pl, nb = (xcd >> 1) * 4 + nl;
  const int c0 = (pl + 2 * nl) & 7;
  if (nb < 14) k1_body<true>(A, smem, pi, nb, c0, r);
  else k1_body<false>(A, smem, pi, nb, c0, r);
}

struct K2Args {
  const u16 *x, *Dk, *wd, *Cre, *Cim, *Lre, *Lim, *ldt;
  char* ws;
  float* out;
};
constexpr int L2_EX = 131072;
constexpr int L2_SIZE = L2_EX + 16384;
constexpr int k2_ex(int s) { return s == 1 ? 1 : (s == 2 ? 1 : (s == 8 ? 1 : (s == 15 ? 5 : 0))); }
constexpr int k2_wait(int s) {
  int n = (s + 1 < 22 ? 4 : 0) + (s + 2 < 22 ? 4 : 0);
  for (int t = s - 3; t < s; ++t)
    if (t >= 0) n += k2_ex(t);
  return n;
}

__device__ __forceinline__ void k2_body(const K2Args& A, char* smem) {
  LAS char* lds = (LAS char*)smem;
  const unsigned lds0 = (unsigned)(uintptr_t)lds;
  const int tid = threadIdx.x, lane = tid & 63;
  const int w = __builtin_amdgcn_readfirstlane(tid >> 6);
  const int t16 = lane & 15, q = lane >> 4;
  const int b = blockIdx.x;
  const int xcd = b & 7, r = b >> 3;
  const int ml = r >> 3, mi = xcd * 4 + ml, nj = r & 7;
  char* ws = A.ws;
  STAMP(0);
  RT0();
  const f32x4 zero4 = {0.f, 0.f, 0.f, 0.f};
  const int r8 = lane >> 3, pc = lane & 7;
  int rotg = ((7 * nj) >> 2) + ((7 * ml) >> 1);
  rotg = rotg >= 14 ? rotg - 14 : rotg;
  const int roty = nj + 2 * ml;

  const unsigned va = w * 2048 + lane * 16;
  const unsigned vw = (16 * w + r8) * 1024 + 16 * (pc ^ (((r8 >> 1) & 3) << 1));
  const unsigned vc = (8 * w + r8) * 1024 + 16 * (pc ^ ((4 * (w & 1) + (r8 >> 1)) & 7));
  auto issue = [&](int s) {
    const unsigned sl = lds0 + (s & 3) * 32768;
    if (s < 14) {
      const int gs = s + rotg >= 14 ? s + rotg - 14 : s + rotg;
      const char* ab = ws + OFF_A + (size_t)(mi * 14 + gs) * 16384;
      const char* wb = (const char*)A.wd + (size_t)gs * 131072 + nj * 128;
      dma(ab, va, sl + w * 2048);
      dma(wb, vw, sl + 16384 + w * 2048);
      dma(ab + 1024, va, sl + w * 2048 + 1024);
      dma(wb + 8192, vw, sl + 16384 + w * 2048 + 1024);
    } else {
      const int ys = (s - 14 + roty) & 7;
      const char* hb = ws + OFF_H + (size_t)(mi * 8 + ys) * 16384;
      dmac(hb, va, sl + w * 2048);
      dma((const char*)A.Cre + nj * 65536 + ys * 128, vc, sl + 16384 + w * 1024);
      dmac(hb + 1024, va, sl + w * 2048 + 1024);
      dma((const char*)A.Cim + nj * 65536 + ys * 128, vc, sl + 24576 + w * 1024);
    }
  };
  const int sig = 8 * nj + w, pi = mi >> 1;
  unsigned plr, pli, pdt;
  {
    const int ch = 8 * sig + (t16 >> 1);
    GLD2(plr, A.Lre + ch); GLD2(pli, A.Lim + ch); GLD2(pdt, A.ldt + ch);
  }
  issue(0);
  f32x4 vv[2][2];
  float car[2], tot[4];
#pragma unroll
  for (int m = 0; m < 4; ++m) {
    const int i = pi - 1 - 4 * m - q;
    const char* p = ws + OFF_TOT + (size_t)((i < 0 ? 0 : i) * 64 + sig) * 64 + t16 * 4;
    GLD4(tot[m], p);
  }
#pragma unroll
  for (int g = 0; g < 2; ++g) {
    const char* p = ws + OFF_CAR + (size_t)(pi * 64 + sig) * 1024 + (8 * (mi & 1) + 4 * g) * 64 + lane * 4;
    GLD4(car[g], p);
  }
#pragma unroll
  for (int g = 0; g < 2; ++g)
#pragma unroll
    for (int j = 0; j < 2; ++j) {
      const char* p = ws + OFF_V + ((size_t)((2 * mi + g) * 64 + sig) * 2 + j) * 1024 + lane * 16;
      GLD16(vv[g][j], p);
    }
  __builtin_amdgcn_s_barrier();
  issue(1);
  __builtin_amdgcn_s_barrier();
  issue(2);
  STAMP(1);
  asm volatile("s_waitcnt vmcnt(22)" : "+v"(plr), "+v"(pli), "+v"(pdt)::"memory");
  f32x2 ca, cf, cp0, cp1, cA4, cAq;
  {
    const float sg = (t16 & 1) ? 1.f : -1.f;
    const f32x2 one = {1.f, 0.f};
    const f32x2 a = ssm_a(plr, pli, pdt);
    const float lr = bf2f(plr), li = bf2f(pli);
    const float nr = a[0] - 1.f, ni = a[1], den = lr * lr + li * li;
    const float fr = (nr * lr + ni * li) / den, fi = (ni * lr - nr * li) / den;
    const f32x2 a8 = csq(csq(csq(a))), a16 = csq(a8), a32 = csq(a16), a64 = csq(a32), A1 = csq(a64), A2 = csq(A1), A4 = csq(A2);
    f32x2 p = cmul((q & 1) ? a8 : one, (q & 2) ? a16 : one);
    p = cmul(p, (mi & 1) ? a64 : one);
    const f32x2 p1 = cmul(p, a32);
    const f32x2 Aq = cmul((q & 1) ? A1 : one, (q & 2) ? A2 : one);
    ca = (f32x2){a[0], sg * a[1]};
    cf = (f32x2){-sg * fr, -fi};
    cp0 = (f32x2){p[0], sg * p[1]};
    cp1 = (f32x2){p1[0], sg * p1[1]};
    cA4 = (f32x2){A4[0], sg * A4[1]};
    cAq = (f32x2){Aq[0], sg * Aq[1]};
  }
  STAMP(2);

  const int kg = w >> 2, wr = w & 1, wc = (w >> 1) & 1;
  const int qq = t16 >> 2, pp = t16 & 3;
  const int trc = (4 * q + qq) * 128 + 32 * ((2 * q + (qq >> 1)) & 3) + 8 * pp + kg * 4096;
  const int trb = trc ^ (wc << 6);
  const int tra = trc ^ (wr << 6);
  const int crb = (32 * wc + t16) * 128 + 16 * (q ^ ((t16 >> 1) & 7)) + kg * 8192;
  const int arb = (kg * 4 + 2 * wr) * 1024 + lane * 16;
  auto trfrag = [&](LAS char* p) -> bf16x8 {
    s16x4 lo = __builtin_amdgcn_ds_read_tr16_b64_v4i16((LAS s16x4*)p);
    s16x4 hi = __builtin_amdgcn_ds_read_tr16_b64_v4i16((LAS s16x4*)(p + 2048));
    return __builtin_shufflevector(lo, hi, 0, 1, 2, 3, 4, 5, 6, 7);
  };
  f32x4 accg[2][2], accy[2][2];
#pragma unroll
  for (int i = 0; i < 2; ++i)
#pragma unroll
    for (int j = 0; j < 2; ++j) { accg[i][j] = zero4; accy[i][j] = zero4; }
  unsigned* flag = (unsigned*)(ws + OFF_FL + mi * 128);
  unsigned pollv = 0;
  u32x2 xv[2], dv[2];
  f32x2 st;
  f32x4 sgv[2], xfv[2], t1v[2];
  float cpn;
  const int orow = 64 * mi + 32 * wr + 8 * qq + 4 * kg + pp;
  const int ocol = 64 * nj + 32 * wc + 4 * q;
  auto scan = [&](int g) {
    const f32x2 cp = g ? cp1 : cp0;
    float h = fmaf(cp[0], cpn, fmaf(cp[1], SWP(cpn), car[g]));
    float o[8];
#pragma unroll
    for (int t = 0; t < 8; ++t) {
      h = fmaf(ca[0], h, fmaf(ca[1], SWP(h), vv[g][t >> 2][t & 3]));
      o[t] = fmaf(cf[0], h, cf[1] * SWP(h));
    }
    const auto s0 = __builtin_amdgcn_permlane16_swap(pk2(o[0], o[1]), pk2(o[4], o[5]), false, false);
    const auto s1 = __builtin_amdgcn_permlane16_swap(pk2(o[2], o[3]), pk2(o[6], o[7]), false, false);
    const u32x4 d = {s0[0], s1[0], s0[1], s1[1]};
    const int c = t16 >> 1;
    const int row = 32 * (2 * (w >> 2) + (t16 & 1)) + 4 * (w & 3) + (c & 3) + 16 * (c >> 2);
    const unsigned vo = row * 128 + 32 * ((2 * g + (q & 1)) ^ ((row >> 1) & 3)) + 16 * (q >> 1);
    const char* sb = ws + OFF_H + (size_t)(mi * 8 + nj) * 16384;
    GST16S(sb, vo, d);
  };
  sfor<0, 22>([&](auto sc) {
    constexpr int s = decltype(sc)::value;
    if (s == 0) {
      VMCNT(18);
    } else if (s == 1) {
      asm volatile("s_waitcnt vmcnt(8)"
                   : "+v"(vv[0][0]), "+v"(vv[0][1]), "+v"(vv[1][0]), "+v"(vv[1][1]), "+v"(car[0]), "+v"(car[1]), "+v"(tot[0]),
                     "+v"(tot[1]), "+v"(tot[2]), "+v"(tot[3])::"memory");
    } else if (s == 5) {
      VMCNT(8);
    } else if (s == 11) {
      asm volatile("s_waitcnt vmcnt(8)" : "+v"(pollv)::"memory");
      unsigned spins = 0;
      while (__builtin_amdgcn_readfirstlane(pollv) < 8u) {
        __builtin_amdgcn_s_sleep(1);
        asm volatile("global_load_dword %0, %1, off sc1\n\ts_waitcnt vmcnt(0)" : "=v"(pollv) : "v"(flag) : "memory");
        if (++spins > (1u << 22)) break;
      }
    } else if (s == 18) {
      asm volatile("s_waitcnt vmcnt(8)" : "+v"(xv[0]), "+v"(xv[1]), "+v"(dv[0]), "+v"(dv[1]), "+v"(st)::"memory");
    } else {
      VMCNT(k2_wait(s));
    }
    BARRIER();
    if (s == 5 && tid == 0) asm volatile("global_atomic_add %0, %1, off" ::"v"(flag), "v"(1u) : "memory");
    LAS char* sl = lds + (s & 3) * 32768;
    bf16x8 af[2][2], bf[2][2];
#pragma unroll
    for (int j = 0; j < 2; ++j) {
#pragma unroll
      for (int e = 0; e < 2; ++e) {
        if (s < 14) {
          af[j][e] = *(LAS bf16x8*)(sl + arb + j * 8192 + e * 1024);
          bf[j][e] = trfrag(sl + 16384 + j * 8192 + (trb ^ (e << 5)));
        } else {
          af[j][e] = trfrag(sl + j * 8192 + (tra ^ (e << 5)));
          bf[j][e] = *(LAS bf16x8*)(sl + 16384 + e * 2048 + (crb ^ (j << 6)));
        }
      }
    }
    SCHED();
    if (s + 3 < 22) issue(s + 3);
    if (s == 8) asm volatile("global_load_dword %0, %1, off sc1" : "=v"(pollv) : "v"(flag) : "memory");
    if (s == 15) {
      const u16* xp = A.x + (size_t)orow * D_ + ocol;
      const u16* xp2 = xp + 16;
      const u16* dp = A.Dk + ocol;
      const u16* dp2 = dp + 16;
      const char* sp = ws + OFF_ST + (size_t)orow * 8;
      GLD8(xv[0], xp); GLD8(xv[1], xp2); GLD8(dv[0], dp); GLD8(dv[1], dp2); GLD8(st, sp);
    }
#pragma unroll
    for (int j = 0; j < 2; ++j)
#pragma unroll
      for (int e = 0; e < 2; ++e)
#pragma unroll
        for (int tt = 0; tt < 2; ++tt) {
          if (s < 14) accg[e][tt] = MFMA(bf[j][e], af[j][tt], accg[e][tt]);
          else accy[e][tt] = MFMA(bf[j][e], af[j][tt], accy[e][tt]);
        }
    SCHED();
    if (s == 1) {
      float sacc = 0.f;
#pragma unroll
      for (int m = 3; m >= 0; --m) {
        const float tv = (pi - 1 - 4 * m - q >= 0) ? tot[m] : 0.f;
        sacc = fmaf(cA4[0], sacc, fmaf(cA4[1], SWP(sacc), tv));
      }
      sacc = fmaf(cAq[0], sacc, cAq[1] * SWP(sacc));
      sacc += __shfl_xor(sacc, 16);
      sacc += __shfl_xor(sacc, 32);
      cpn = sacc;
      scan(0);
    }
    if (s == 2) scan(1);
    if (s == 13) {
#pragma unroll
      for (int e = 0; e < 2; ++e) *(LAS f32x4*)(lds + L2_EX + (w * 2 + e) * 1024 + lane * 16) = kg ? accg[e][0] : accg[e][1];
    }
    if (s == 14 || s == 15) {
      const int e = s & 1;
      const f32x4 pv = *(LAS f32x4*)(lds + L2_EX + ((w ^ 4) * 2 + e) * 1024 + lane * 16);
#pragma unroll
      for (int rr = 0; rr < 4; ++rr) {
        const float g = rbf((kg ? accg[e][1][rr] : accg[e][0][rr]) + pv[rr]);
        sgv[e][rr] = rbf(g / (1.f + __expf(-g)));
      }
    }
    if (s == 18 || s == 19) {
      const int e = s & 1;
#pragma unroll
      for (int rr = 0; rr < 4; ++rr) {
        const float xx = bf2f((xv[e][rr >> 1] >> (16 * (rr & 1))) & 0xffff);
        const float dd = bf2f((dv[e][rr >> 1] >> (16 * (rr & 1))) & 0xffff);
        const float xn = rbf(rbf(xx - st[0]) / st[1]);
        xfv[e][rr] = fmaf(rbf(xn * dd), sgv[e][rr], xx);
        t1v[e][rr] = 2.f * sgv[e][rr];
      }
    }
  });
#pragma unroll
  for (int e = 0; e < 2; ++e) *(LAS f32x4*)(lds + L2_EX + (w * 2 + e) * 1024 + lane * 16) = kg ? accy[e][0] : accy[e][1];
  BARRIER();
#pragma unroll
  for (int e = 0; e < 2; ++e) {
    const f32x4 pv = *(LAS f32x4*)(lds + L2_EX + ((w ^ 4) * 2 + e) * 1024 + lane * 16);
    f32x4 o;
#pragma unroll
    for (int rr = 0; rr < 4; ++rr) o[rr] = fmaf((kg ? accy[e][1][rr] : accy[e][0][rr]) + pv[rr], t1v[e][rr], xfv[e][rr]);
    *(f32x4*)(A.out + (size_t)orow * D_ + ocol + 16 * e) = o;
  }
  STAMP(11);
  RT1(1, b * 8 + w, ws);
}

__global__ __launch_bounds__(512, 2) NOPK void k2(K2Args A) {
  extern __shared__ __attribute__((aligned(16))) char smem[];
  k2_body(A, smem);
}

extern "C" void kernel_launch(void* const* d_in, const int* in_sizes, int n_in, void* d_out, int out_size, void* d_ws,
                              size_t ws_size, hipStream_t stream) {
  K1Args a;
  a.x = (const u16*)d_in[0]; a.Lre = (const u16*)d_in[1]; a.Lim = (const u16*)d_in[2];
  a.Bre = (const u16*)d_in[3]; a.Bim = (const u16*)d_in[4]; a.Cre = (const u16*)d_in[5]; a.Cim = (const u16*)d_in[6];
  a.ldt = (const u16*)d_in[7]; a.wg = (const u16*)d_in[10]; a.wu = (const u16*)d_in[11]; a.wd = (const u16*)d_in[12];
  a.ws = (char*)d_ws;
  (void)hipFuncSetAttribute((const void*)k1, hipFuncAttributeMaxDynamicSharedMemorySize, L1_SIZE);
  (void)hipFuncSetAttribute((const void*)k2, hipFuncAttributeMaxDynamicSharedMemorySize, L2_SIZE);
  k1<<<256, 512, L1_SIZE, stream>>>(a);
  K2Args b;
  b.x = (const u16*)d_in[0]; b.Dk = (const u16*)d_in[8]; b.wd = (const u16*)d_in[12];
  b.Cre = (const u16*)d_in[5]; b.Cim = (const u16*)d_in[6];
  b.Lre = (const u16*)d_in[1]; b.Lim = (const u16*)d_in[2]; b.ldt = (const u16*)d_in[7];
  b.ws = (char*)d_ws; b.out = (float*)d_out;
  k2<<<256, 512, L2_SIZE, stream>>>(b);
}
```

```cpp
#include <hip/hip_runtime.h>
#include <hip/hip_bf16.h>
#include <stdint.h>

typedef unsigned short u16;
typedef __attribute__((ext_vector_type(8))) short bf16x8;
typedef __attribute__((ext_vector_type(4))) short s16x4;
typedef __attribute__((ext_vector_type(16))) float f32x16;
typedef __attribute__((ext_vector_type(4))) float f32x4;
typedef __attribute__((ext_vector_type(2))) float f32x2;
typedef __attribute__((ext_vector_type(4))) unsigned u32x4;
typedef __attribute__((ext_vector_type(2))) unsigned u32x2;
typedef __attribute__((ext_vector_type(2))) __bf16 bf16x2_t;
#define LAS __attribute__((address_space(3)))
#if defined(__HIP_DEVICE_COMPILE__)
#define NOPK __attribute__((target("no-packed-fp32-ops")))
#else
#define NOPK
#endif

constexpr int D_ = 512;

constexpr size_t OFF_A   = 0;
constexpr size_t OFF_H   = OFF_A + (size_t)32 * 112 * 2048;
constexpr size_t OFF_CAR = OFF_H + (size_t)32 * 32 * 4096;
constexpr size_t OFF_TOT = OFF_CAR + (size_t)16 * 1024 * 64;
constexpr size_t OFF_AT  = OFF_TOT + (size_t)16 * 4096;
constexpr size_t OFF_ST  = OFF_AT + 8192;

constexpr int L1_AGG = 131072;
constexpr int L1_AC  = L1_AGG + 18432;
constexpr int L1_STF = L1_AC + 4608;
constexpr int L1_ST  = L1_STF + 2048;
constexpr int L1_CS  = L1_ST + 2048;
constexpr int L1_CSR = L1_AC;
constexpr int L1_DUM = L1_CS + 4608;
constexpr int L1_SIZE = L1_DUM + 1024;

__device__ __forceinline__ float bf2f(unsigned h) { return __builtin_bit_cast(float, h << 16); }
__device__ __forceinline__ float rbf(float v) { return (float)(__bf16)v; }
__device__ __forceinline__ unsigned pk2(float lo, float hi) {
  bf16x2_t r = {(__bf16)lo, (__bf16)hi};
  return __builtin_bit_cast(unsigned, r);
}
template <int CTRL> __device__ __forceinline__ float dpp0(float x) {
  return __builtin_bit_cast(float, __builtin_amdgcn_update_dpp(0, __builtin_bit_cast(int, x), CTRL, 0xf, 0xf, true));
}
#define SWP(x) dpp0<0xB1>(x)
__device__ __forceinline__ f32x2 csq(f32x2 a) { return (f32x2){fmaf(a[0], a[0], -a[1] * a[1]), 2.f * a[0] * a[1]}; }
__device__ __forceinline__ f32x2 cmul(f32x2 a, f32x2 b) {
  return (f32x2){fmaf(a[0], b[0], -a[1] * b[1]), fmaf(a[0], b[1], a[1] * b[0])};
}
__device__ __forceinline__ f32x2 cfma(f32x2 a, f32x2 b, f32x2 c) {
  return (f32x2){fmaf(a[0], b[0], fmaf(-a[1], b[1], c[0])), fmaf(a[0], b[1], fmaf(a[1], b[0], c[1]))};
}
template <int V> struct IC { static constexpr int value = V; };
template <int I, int N, class F> __device__ __forceinline__ void sfor(F&& f) {
  if constexpr (I < N) { f(IC<I>{}); sfor<I + 1, N>(f); }
}
__device__ __forceinline__ const char* uptr(const void* base, unsigned off) {
  return (const char*)base + (size_t)(unsigned)__builtin_amdgcn_readfirstlane(off);
}
#define MFMA16(a, b, c) __builtin_amdgcn_mfma_f32_16x16x32_bf16(a, b, c, 0, 0, 0)
#define MFMA32(a, b, c) __builtin_amdgcn_mfma_f32_32x32x16_bf16(a, b, c, 0, 0, 0)
#define SCHED() __builtin_amdgcn_sched_barrier(0)
#define LP(T, a) ((LAS T*)(unsigned)(a))
#define AI __attribute__((always_inline))
#define LMASK(m) __builtin_amdgcn_inverse_ballot_w64(m)
#define LB0 LMASK(0xAAAAAAAAAAAAAAAAull)
#define LB1 LMASK(0xCCCCCCCCCCCCCCCCull)
#define LB3 LMASK(0xFF00FF00FF00FF00ull)
#define LB4 LMASK(0xFFFF0000FFFF0000ull)
#define LLO LMASK(0x00000000FFFFFFFFull)

#define GLD16(dst, p) asm volatile("global_load_dwordx4 %0, %1, off" : "=v"(dst) : "v"(p) : "memory")
#define GLD8(dst, p) asm volatile("global_load_dwordx2 %0, %1, off" : "=v"(dst) : "v"(p) : "memory")
#define GLD2(dst, p) asm volatile("global_load_ushort %0, %1, off" : "=v"(dst) : "v"(p) : "memory")
#define GLD16S(dst, vo, sb) asm volatile("global_load_dwordx4 %0, %1, %2" : "=v"(dst) : "v"(vo), "s"(sb) : "memory")
#define GLD8S(dst, vo, sb) asm volatile("global_load_dwordx2 %0, %1, %2" : "=v"(dst) : "v"(vo), "s"(sb) : "memory")
#define GST16S(sb, vo, v) asm volatile("global_store_dwordx4 %0, %1, %2 sc0 sc1\n\ts_nop 1" ::"v"(vo), "v"(v), "s"(sb) : "memory")
#define LDSW(a, off, v) asm volatile("ds_write_b128 %0, %1 offset:%2" ::"v"(a), "v"(v), "i"(off) : "memory")
#define VMCNT(n) asm volatile("s_waitcnt vmcnt(%0)" ::"i"(n) : "memory")
#define BARRIER() do { asm volatile("s_waitcnt lgkmcnt(0)" ::: "memory"); __builtin_amdgcn_s_barrier(); asm volatile("" ::: "memory"); } while (0)
#define RBAR(s, n) do { asm volatile("s_waitcnt vmcnt(%0) lgkmcnt(0) ; NEED %1" ::"i"(n), "i"(s) : "memory"); __builtin_amdgcn_s_barrier(); asm volatile("" ::: "memory"); } while (0)
__device__ __forceinline__ void dma(const char* sbase, unsigned voff, unsigned lds_dst) {
  asm volatile("s_mov_b32 m0, %2\n\ts_nop 0\n\tglobal_load_lds_dwordx4 %0, %1" ::"v"(voff), "s"(sbase), "s"(lds_dst) : "memory");
}
template <int S> __device__ __forceinline__ void dmar(const char* sbase, unsigned voff, unsigned lds_dst) {
  asm volatile("s_mov_b32 m0, %2\n\ts_nop 0\n\tglobal_load_lds_dwordx4 %0, %1 ; RING %3" ::"v"(voff), "s"(sbase), "s"(lds_dst), "i"(S) : "memory");
}
__device__ __forceinline__ bf16x8 trfrag2(unsigned a, unsigned b) {
  s16x4 lo = __builtin_amdgcn_ds_read_tr16_b64_v4i16(LP(s16x4, a));
  s16x4 hi = __builtin_amdgcn_ds_read_tr16_b64_v4i16(LP(s16x4, b));
  return __builtin_shufflevector(lo, hi, 0, 1, 2, 3, 4, 5, 6, 7);
}
__device__ __forceinline__ bf16x8 trfrag(unsigned a, int d) {
  s16x4 lo = __builtin_amdgcn_ds_read_tr16_b64_v4i16(LP(s16x4, a));
  s16x4 hi = __builtin_amdgcn_ds_read_tr16_b64_v4i16(LP(s16x4, a + d));
  return __builtin_shufflevector(lo, hi, 0, 1, 2, 3, 4, 5, 6, 7);
}

#define STAMP(i)
constexpr size_t OFF_DBG = (size_t)64 << 20;
#define RT0()
#define RT1(a,b,c)

struct K1Args {
  const u16 *x, *Lre, *Lim, *Bre, *Bim, *Cre, *Cim, *ldt, *wg, *wu, *wd;
  char* ws;
};

constexpr int k1_bar_unit(int s) { return 16 * (s - 8) - 3; }
constexpr int k1_ival(int u) { return 8 + (u + 3) / 16; }
constexpr int k1_nring(int s) { return s > 14 ? 0 : (s < 8 ? 3 : 4); }
constexpr int k1_dma_unit(int t, int d) { return t == 8 ? (d == 0 ? 1 : 4 * d - 2) : k1_bar_unit(t) + 1 + 4 * d; }
#define PFS 128
constexpr int K1_NPF = (90112 / (64 * PFS) + 7) / 8;
constexpr int k1_pf_unit(int n) { return 64 + 4 * n; }
constexpr int k1_s_unit(int p, int hq) { return p == 0 ? 10 + (2 * hq) / 3 : (p == 3 ? 97 + hq / 2 : 32 * p + 1 + (2 * hq) / 3); }
constexpr int k1_extra(bool gt, int u) {
  int n = 0;
  for (int i = 0; i < K1_NPF; ++i) n += k1_pf_unit(i) == u;
  if (gt) n += (u == 53) + (u == 57) + (u == 75);
  else
    for (int p = 0; p < 3; ++p) n += (k1_s_unit(p, 29) == u) + (k1_s_unit(p, 31) == u);
  return n;
}
constexpr int k1_wait(bool gt, int s) {
  int n = k1_nring(s + 1) + k1_nring(s + 2);
  for (int u = s >= 11 ? k1_dma_unit(s - 3, 3) : 0; u < k1_bar_unit(s); ++u) n += k1_extra(gt, u);
  return n;
}

template <bool GT>
__device__ __forceinline__ void k1_body(const K1Args& A, char* smem, int pi, int nb, int c0, int rx, unsigned long long cpc,
                                        unsigned clen) {
  const int tid = threadIdx.x, lane = tid & 63;
  const int w = __builtin_amdgcn_readfirstlane(tid >> 6) & 7;
  const int rg = w & 3, cg = w >> 2, X = rg ^ cg;
  STAMP(0);
  RT0();
  const unsigned lds0 = (unsigned)(uintptr_t)(LAS char*)smem;
  const int r8 = lane >> 3, pc = lane & 7;
  const int tb0 = GT ? nb - 8 : 14 + 9 * (nb - 14);
  constexpr int SSLOT = GT ? 8 : 14;
  auto olane = [&]() AI -> int { int l = lane; asm volatile("" : "+v"(l)); return l; };

  const char* xb = uptr(A.x, pi * 131072 + w * 16384);
  const unsigned vs = r8 * 1024 + 16 * (pc ^ ((r8 >> 1) | ((w & 1) << 2)));
  const unsigned vw = GT ? (8 * w + r8) * 3584 + 16 * (pc ^ (((r8 >> 1) & 1) << 2)) : vs;
  const char* bmat = uptr((w & 1) ? A.Bim : A.Bre, ((GT ? nb : tb0) * 16 + 8 * ((w >> 1) & 1)) * 1024);
  auto wbase = [&](int p, int c) AI -> const char* {
    if (GT) return (const char*)((p & 1) ? A.wu : A.wg) + (size_t)c * (64 * 3584) + (128 * nb + 64 * (p >> 1)) * 2;
    return bmat + (unsigned)((4 * cg + p) * 16384 + c * 128);
  };
  auto issue = [&](auto sc, int d) AI {
    constexpr int s = decltype(sc)::value;
    const unsigned dst = lds0 + (s & 3) * 32768;
    if constexpr (s < 8) {
      const int c = (c0 + s) & 7;
      if (d == 0) dmar<s>(xb + c * 128, vs, dst + w * 2048);
      if (d == 1) dmar<s>(wbase(0, c), vw, dst + 16384 + w * 1024);
      if (d == 2) dmar<s>(xb + c * 128 + 8192, vs, dst + w * 2048 + 1024);
    } else if constexpr (s == SSLOT) {
      const int i = 4 * cg + d;
      dmar<s>(uptr(bmat, (GT ? 0 : 8 * 16384) + ((c0 + i) & 7) * 128), vs, dst + i * 4096 + rg * 1024);
    } else if constexpr (s < 15) {
      constexpr int sp = GT ? s - 9 : s - 8;
      dmar<s>(wbase(1 + (sp >> 1), (c0 + 4 * (sp & 1) + d) & 7), vw, dst + d * 8192 + w * 1024);
    }
  };
  auto prefetch = [&](int n) AI {
    const unsigned o0 = (n * 8 + w) * (64 * PFS);
    const unsigned o = o0 < 90112 ? o0 : 0, stride = o0 < 90112 ? PFS : 0;
    const uint64_t b0 = (uint64_t)A.wd + rx * 57344, b1 = (uint64_t)A.Cre + rx * 16384 - 57344, b2 = (uint64_t)A.Cim + rx * 16384 - 73728;
    const uint64_t b12 = o < 73728 ? b1 : b2;
    const uint64_t sb = (o < 57344 ? b0 : b12) + o;
    const unsigned vo = olane() * stride;
    asm volatile("s_mov_b32 m0, %2\n\ts_nop 0\n\tglobal_load_lds_dword %0, %1" ::"v"(vo), "s"(sb), "s"(lds0 + L1_DUM) : "memory");
  };

  {
    unsigned off = ((rx & 1) * 8 + w) * 4096 + lane * 64;
    off = off < clen ? off : 0;
    asm volatile("s_mov_b32 m0, %2\n\ts_nop 0\n\tglobal_load_lds_dword %0, %1" ::"v"(off), "s"(cpc), "s"(lds0 + L1_DUM) : "memory");
  }
  unsigned plr, pli, pdt;
  const int tsp = GT ? 8 : ((tid >> 5) < 8 ? (tid >> 5) : 8);
  {
    const int ch = 16 * (tb0 + tsp) + ((tid & 31) >> 1);
    GLD2(plr, A.Lre + ch); GLD2(pli, A.Lim + ch); GLD2(pdt, A.ldt + ch);
  }
  issue(IC<0>{}, 0); issue(IC<0>{}, 1); issue(IC<0>{}, 2);
  __builtin_amdgcn_s_barrier();
  issue(IC<1>{}, 0); issue(IC<1>{}, 1); issue(IC<1>{}, 2);
  __builtin_amdgcn_s_barrier();
  issue(IC<2>{}, 0); issue(IC<2>{}, 1); issue(IC<2>{}, 2);
  STAMP(1);
  char* ws = A.ws;
  {
    asm volatile("s_waitcnt vmcnt(9)" : "+v"(plr), "+v"(pli), "+v"(pdt)::"memory");
    const float lr = bf2f(plr), li = bf2f(pli);
    const float dt = rbf(expf(bf2f(pdt)));
    float sn, cs;
    sincosf(dt * li, &sn, &cs);
    const float e = expf(dt * lr);
    const float ar = e * cs, ai = e * sn;
    const float nr = ar - 1.f, den = lr * lr + li * li;
    const float fr = (nr * lr + ai * li) / den, fi = (ai * lr - nr * li) / den;
    const bool im = tid & 1;
    const f32x4 ac = {ar, im ? -ai : ai, im ? -fr : fr, -fi};
    *LP(f32x4, lds0 + L1_AC + (tsp * 32 + (tid & 31)) * 16) = ac;
    if (pi == 0 && tid < (GT ? 32 : 288))
      *(f32x2*)(ws + OFF_AT + (size_t)((tb0 + tsp) * 32 + (tid & 31)) * 8) = (f32x2){ac[0], ac[1]};
  }

  const int n = lane & 31, h = lane >> 5, t16 = lane & 15;
  const int rho = 16 * ((n >> 2) & 1) + 4 * (n >> 3) + (n & 3);
  const int tau = rho ^ (8 * cg);
  const unsigned selw = (t16 == 4 * ((lane >> 4) & 1)) ? 0x3F803F80u : 0u;
  const bf16x8 sel = __builtin_bit_cast(bf16x8, (u32x4){selw, selw, selw, selw});
  const unsigned axb = lds0 + (32 * rg + tau) * 128 + 16 * ((2 * X + h) ^ (((tau >> 1) & 3) | ((tau >> 4) << 2)));
  auto sbase = [&](int l) AI -> unsigned {
    const int nn = l & 31, hh = l >> 5;
    return lds0 + (16 * (nn >> 4) + 8 * (nn & 1) + ((nn >> 1) & 7)) * 128 + 16 * ((2 * X + hh) ^ (((nn >> 2) & 3) | ((nn & 1) << 2)));
  };
  unsigned aw[4];
  {
    const unsigned b = GT ? lds0 + (16 * X + 8 * h + (t16 >> 2)) * 128 + 32 * ((2 * cg + ((lane >> 4) & 1)) ^ ((t16 >> 3) << 1)) + 8 * (t16 & 3)
                          : sbase(lane) + cg * 4096;
#pragma unroll
    for (int j = 0; j < 4; ++j) aw[j] = b ^ (GT ? j << 11 : j << 5);
  }
  auto flip = [&]() AI {
#pragma unroll
    for (int j = 0; j < 4; ++j) aw[j] ^= 65536;
    asm volatile("" : "+v"(aw[0]), "+v"(aw[1]), "+v"(aw[2]), "+v"(aw[3]));
  };
  auto wfrag = [&](unsigned a) AI -> bf16x8 {
    if (GT) return trfrag(a, 512);
    return *LP(bf16x8, a);
  };
  auto mm = [&](bf16x8 wfv, bf16x8 xfv, f32x16 c) AI -> f32x16 { return GT ? MFMA32(wfv, xfv, c) : MFMA32(xfv, wfv, c); };

  const f32x4 zero4 = {0.f, 0.f, 0.f, 0.f};
  f32x16 zero16;
#pragma unroll
  for (int i = 0; i < 16; ++i) zero16[i] = 0.f;
  f32x16 acc[4], accs, gr = zero16;
  f32x4 sm = zero4, csa = zero4;
  acc[0] = zero16;
  bf16x8 xf[32];
  bf16x8 wd2, wd3;
  auto p1tail = [&](int i) AI {
    gr = MFMA32(xf[4 * i + 2], xf[4 * i + 2], gr);
    sm = MFMA16(sel, xf[4 * i + 2], sm);
    acc[0] = mm(wd2, xf[4 * i + 2], acc[0]);
    acc[0] = mm(wd3, xf[4 * i + 3], acc[0]);
  };
  sfor<0, 8>([&](auto ic) AI {
    constexpr int i = decltype(ic)::value;
    RBAR(i, k1_wait(GT, i));
    if (i == 0) STAMP(2);
    if (i == 4) STAMP(3);
    constexpr int so = (i & 1) * 32768;
    const unsigned ax = axb + (i & 2) * 32768;
    xf[4 * i] = *LP(bf16x8, ax + so);
    const bf16x8 w0 = wfrag(aw[0] + so + 16384);
    xf[4 * i + 1] = *LP(bf16x8, (ax ^ 32) + so);
    const bf16x8 w1 = wfrag(aw[1] + so + 16384);
    xf[4 * i + 2] = *LP(bf16x8, (ax ^ 64) + so);
    const bf16x8 w2 = wfrag(aw[2] + so + 16384);
    xf[4 * i + 3] = *LP(bf16x8, (ax ^ 96) + so);
    const bf16x8 w3 = wfrag(aw[3] + so + 16384);
    if (i & 1) flip();
    SCHED();
    if (i > 0) { gr = MFMA32(xf[4 * i - 2], xf[4 * i - 2], gr); SCHED(); }
    issue(IC<i + 3>{}, 0);
    SCHED();
    if (i > 0) {
      acc[0] = mm(wd2, xf[4 * i - 2], acc[0]);
      sm = MFMA16(sel, xf[4 * i - 2], sm);
      acc[0] = mm(wd3, xf[4 * i - 1], acc[0]);
      SCHED();
    }
    issue(IC<i + 3>{}, 1);
    SCHED();
    gr = MFMA32(xf[4 * i], xf[4 * i], gr);
    sm = MFMA16(sel, xf[4 * i], sm);
    acc[0] = mm(w0, xf[4 * i], acc[0]);
    SCHED();
    issue(IC<i + 3>{}, 2);
    SCHED();
    csa = MFMA16(sel, w0, csa);
    acc[0] = mm(w1, xf[4 * i + 1], acc[0]);
    SCHED();
    if (i + 3 >= 8) issue(IC<i + 3>{}, 3);
    wd2 = w2; wd3 = w3;
    SCHED();
  });

  float nmb, sdv, rsg;
  float dgt[4];
  f32x2 stp;
  f32x4 cq[2];
  f32x4 mq[2], rq[2], acv;
  float csv, zc[2], yv[2], tg;
  unsigned ob[8];
  f32x4 pv[2], csp, stq;
  auto cs_put = [&](int ts) AI {
    const int l = olane();
    if (LLO) *LP(float, lds0 + L1_CS + ts * 512 + X * 4 + l * 16) = csa[0];
  };
  auto cs_get = [&](int ts) AI { csp = *LP(f32x4, lds0 + L1_CS + ts * 512 + (olane() & 31) * 16); };
  auto cs_red = [&]() AI {
    *LP(float, lds0 + L1_CSR + w * 128 + (olane() & 31) * 4) = (csp[0] + csp[1]) + (csp[2] + csp[3]);
  };
  auto stat_q = [&](int k) AI {
    const int l = olane();
    if (k < 2) {
#pragma unroll
      for (int a = 2 * k; a < 2 * k + 2; ++a) {
        float g0 = gr[4 * a], g1 = gr[4 * a + 1], g2 = gr[4 * a + 2], g3 = gr[4 * a + 3];
        asm volatile("" : "+v"(g0), "+v"(g1), "+v"(g2), "+v"(g3));
        const float lo = LB0 ? g1 : g0, hi = LB0 ? g3 : g2;
        dgt[a] = LB1 ? hi : lo;
        asm volatile("" : "+v"(dgt[a]));
      }
    } else if (k == 2) {
      const float lo = LB3 ? dgt[1] : dgt[0], hi = LB3 ? dgt[3] : dgt[2];
      const float dg = LB4 ? hi : lo;
      const unsigned a = lds0 + L1_ST + (32 * rg + tau) * 16 + cg * 8;
      if (LMASK(0xF0F0F0F00F0F0F0Full)) *LP(float, a) = dg;
      if (LLO) *LP(float, a + 4) = sm[0];
    } else if (k == 3) {
      cs_put(4 * cg);
    } else if (k == 5) {
      stq = *LP(f32x4, lds0 + L1_ST + (32 * rg + tau) * 16);
    } else if (k == 6) {
      const float mean = (stq[1] + stq[3]) * (1.f / 512.f);
      const float var = fmaxf(fmaf(stq[0] + stq[2], 1.f / 512.f, -mean * mean), 0.f);
      stp = (f32x2){rbf(mean), var};
    } else if (k == 7) {
      stp[1] = rbf(rbf(stp[1]) + 1e-5f);
    } else if (k == 8) {
      sdv = rbf(__builtin_sqrtf(stp[1]));
      nmb = -stp[0];
    } else if (k == 9) {
      const float rs = __builtin_amdgcn_rcpf(sdv);
      rsg = rs * -1.44269504088896f;
      *LP(float, lds0 + L1_STF + w * 256 + (l >> 5) * 128 + rho * 4) = (l >> 5) ? rs : nmb;
      if (nb == 0 && cg == 0 && l < 32) *(f32x2*)(ws + OFF_ST + (size_t)(pi * 128 + rg * 32 + tau) * 8) = (f32x2){stp[0], sdv};
    }
  };
  auto cq_load = [&](int ts, int a) AI { cq[a & 1] = *LP(f32x4, lds0 + L1_CSR + w * 128 + a * 32 + (olane() >> 5) * 16); };
  auto gate_q = [&](int p, int hq) AI {
    const int v = hq >> 1;
    if ((hq & 7) == 4 && hq < 24) cq_load(4 * cg + p, (hq >> 3) + 1);
    if (!(hq & 1)) {
      tg = __builtin_amdgcn_exp2f(fmaf(nmb, cq[(v >> 2) & 1][v & 3], acc[p][v]) * rsg);
    } else {
      float sv = __builtin_amdgcn_rcpf(fmaf(tg, sdv, sdv));
      asm volatile("" : "+v"(sv));
      acc[p][v] = sv;
    }
  };
  auto up_q = [&](int p, int k) AI {
    if (!(k & 1) && k < 6) cq_load(4 * cg + p, (k >> 1) + 1);
    const int v = 2 * k;
    const float h0 = fmaf(nmb, cq[(v >> 2) & 1][v & 3], acc[p][v]) * acc[p - 1][v];
    const float h1 = fmaf(nmb, cq[(v >> 2) & 1][(v & 3) + 1], acc[p][v + 1]) * acc[p - 1][v + 1];
    ob[k] = pk2(h0, h1);
  };
  auto up_st = [&](int p, int e) AI {
    const u32x4 o = {ob[4 * e], ob[4 * e + 1], ob[4 * e + 2], ob[4 * e + 3]};
    const char* sb = uptr(ws, (unsigned)OFF_A + (((2 * pi + (rg >> 1)) * 112 + 8 * nb + 4 * (p >> 1) + 2 * cg + e) * 2 + (rg & 1)) * 1024);
    const unsigned vo = (tau + (olane() & 32)) * 16;
    GST16S(sb, vo, o);
  };
  auto ssm_pre = [&](int ts, float scale) AI {
    const int l = olane();
    acv = *LP(f32x4, lds0 + L1_AC + (ts * 32 + (l & 31)) * 16);
    csp = *LP(f32x4, lds0 + L1_CS + ts * 512 + (l & 31) * 16);
    csv = (csp[0] + csp[1]) + (csp[2] + csp[3]);
  };
  auto ssm_mr = [&](int c, int g) AI {
    const unsigned a = lds0 + L1_STF + w * 256 + (olane() >> 5) * 64 + (8 * c + 4 * g) * 4;
    mq[c] = *LP(f32x4, a);
    rq[c] = *LP(f32x4, a + 128);
  };
  auto ssm_a = [&](float av, int c, int i) AI {
    const float t = fmaf(mq[c][i & 3], csv, av);
    yv[c] = fmaf(acv[2], t, SWP(t) * acv[3]);
  };
  auto ssm_b = [&](int c, int i) AI {
    const float zo = zc[c];
    zc[c] = i == 0 ? rq[c][0] * yv[c] : fmaf(rq[c][i & 3], yv[c], fmaf(acv[0], zo, SWP(zo) * acv[1]));
    if (i & 1) ob[4 * c + (i >> 1)] = pk2(zo, zc[c]);
  };
  auto ssm_st = [&](int ts, int c) AI {
    const int l = olane();
    const u32x4 o = {ob[4 * c], ob[4 * c + 1], ob[4 * c + 2], ob[4 * c + 3]};
    const char* sb = uptr(ws, (unsigned)OFF_H + (((2 * pi + (rg >> 1)) * 32 + tb0 + ts) * 8 + 4 * (rg & 1) + (c ^ cg)) * 512);
    const unsigned vo = ((l & 31) ^ (c ^ cg) ^ ((l >> 5) << 3)) * 16 + (l >> 5) * 1024;
    GST16S(sb, vo, o);
    *LP(float, lds0 + L1_AGG + ts * 2048 + (l & 31) * 64 + ((rg ^ ((l >> 2) & 3)) * 16) + (2 * (l >> 5) + (c ^ cg)) * 4) = zc[c];
  };
  auto ssm_q = [&](int p, int hq) AI {
    const int c = (hq >> 1) & 1, i = hq >> 2;
    if (!(hq & 1)) ssm_a(acc[p][8 * c + i], c, i);
    else {
      ssm_b(c, i);
      if (i == 3) ssm_mr(c, 1);
      if (i == 7) ssm_st(4 * cg + p, c);
    }
  };
  const unsigned exs = lds0 + (GT ? L1_AGG : 0);
  auto ss_send = [&](int g) AI {
    *LP(f32x4, exs + w * 2048 + g * 1024 + olane() * 16) = (f32x4){accs[8 + 4 * g], accs[9 + 4 * g], accs[10 + 4 * g], accs[11 + 4 * g]};
  };
  auto ss_recv = [&]() AI {
    const unsigned a = exs + (w ^ 4) * 2048 + olane() * 16;
    pv[0] = *LP(f32x4, a);
    pv[1] = *LP(f32x4, a + 1024);
  };
  auto ss_q = [&](int hq) AI {
    const int i = hq >> 1;
    if (!(hq & 1)) ssm_a(accs[i] + pv[i >> 2][i & 3], 0, i);
    else {
      ssm_b(0, i);
      if (i == 3) ssm_mr(0, 1);
    }
  };
  f32x4 pz[4];
  f32x2 pa;
  float pcv;
  auto pfx_load = [&](int ts) AI {
    const int l = olane();
    pa = *LP(f32x2, lds0 + L1_AC + (ts * 32 + (l & 31)) * 16);
    const unsigned a = lds0 + L1_AGG + ts * 2048 + (l & 31) * 64 + ((l >> 2) & 3) * 16;
#pragma unroll
    for (int g = 0; g < 4; ++g) pz[g] = *LP(f32x4, a ^ (g * 16));
  };
  auto pfx_step = [&](int k) AI {
    if (k < 3) pa = csq(pa);
    else {
      const int s = k - 3;
      const float z = pz[s >> 2][s & 3];
      pz[s >> 2][s & 3] = s == 0 ? 0.f : pcv;
      pcv = s == 0 ? z : fmaf(pa[0], pcv, z) + SWP(pcv) * pa[1];
    }
  };
  auto pfx_store = [&](int ts) AI {
    const int cp = (tb0 + ts) * 32 + (olane() & 31);
    char* p = ws + OFF_CAR + (size_t)pi * 65536 + (cp & 1) * 8192 + (cp >> 1) * 16;
#pragma unroll
    for (int g = 0; g < 4; ++g) *(f32x4*)(p + g * 16384) = pz[g];
    *(float*)(ws + OFF_TOT + (size_t)(pi * 1024 + cp) * 4) = pcv;
  };
  auto quantum = [&](auto uc) AI {
    constexpr int u = decltype(uc)::value;
    if constexpr (u < 10) stat_q(u);
    if constexpr (GT) {
      if constexpr (u == 4) cs_get(4 * cg);
      if constexpr (u == 45) cs_get(4 * cg + 1);
      if constexpr (u == 77) cs_get(4 * cg + 2);
      if constexpr (u == 110) cs_get(4 * cg + 3);
      if constexpr (u == 6 || u == 46 || u == 78 || u == 111) cs_red();
      if constexpr (u == 8) cq_load(4 * cg, 0);
      if constexpr (u >= 10 && u < 42) gate_q(0, u - 10);
      if constexpr (u == 18 || u == 19) ss_send(u - 18);
      if constexpr (u == 47) cq_load(4 * cg + 1, 0);
      if constexpr (u >= 49 && u < 57) up_q(1, u - 49);
      if constexpr (u == 53) up_st(1, 0);
      if constexpr (u == 57) { up_st(1, 1); ssm_pre(8, 0.5f); ss_recv(); ssm_mr(0, 0); }
      if constexpr (u >= 59 && u < 75) ss_q(u - 59);
      if constexpr (u == 75) ssm_st(8, 0);
      if constexpr (u == 79) { cq_load(4 * cg + 2, 0); pfx_load(8); }
      if constexpr (u >= 81) gate_q(2, u - 81);
      if constexpr (u >= 82 && u < 101) pfx_step(u - 82);
      if constexpr (u == 102) { if (w == 0 && olane() < 32) pfx_store(8); }
    } else {
      sfor<0, 4>([&](auto pc_) AI {
        constexpr int p = decltype(pc_)::value;
        if constexpr (u == k1_s_unit(p, 0) - (p > 0 ? 1 : 0)) { ssm_pre(4 * cg + p, 1.f); ssm_mr(0, 0); ssm_mr(1, 0); }
        sfor<0, 32>([&](auto hc) AI {
          constexpr int hq = decltype(hc)::value;
          if constexpr (u == k1_s_unit(p, hq)) ssm_q(p, hq);
        });
      });
    }
  };

  unsigned as0, as2;
  bf16x8 f[4];
  auto rd = [&](auto uc) AI {
    constexpr int u = decltype(uc)::value;
    if constexpr (u < 112) {
      constexpr int s = 8 + (u >> 4), k = u & 15, so = (s & 1) * 32768;
      if constexpr (s == SSLOT) {
        if (k == 0) {
          as0 = sbase(olane()) + (s & 2) * 32768;
          as2 = as0 ^ 64;
        }
        f[u & 3] = *LP(bf16x8, ((k & 1) ? as2 : as0) + so + (k >> 1) * 4096);
      } else {
        f[u & 3] = wfrag(aw[k & 3] + so + (k >> 2) * 8192);
      }
      if ((s & 1) && k == 15 && s < 14) flip();
    }
  };
  RBAR(8, k1_wait(GT, 8));
  STAMP(4);
  rd(IC<0>{}); rd(IC<1>{}); rd(IC<2>{}); rd(IC<3>{});
  SCHED();
  p1tail(7);
  f32x4 csb = zero4;
  SCHED();
  sfor<0, 112>([&](auto uc) AI {
    constexpr int u = decltype(uc)::value;
    constexpr int s = 8 + (u >> 4), k = u & 15;
    constexpr bool ss = s == SSLOT;
    constexpr int sp = GT ? s - 9 : s - 8, p = 1 + (sp >> 1), half = sp & 1;
    constexpr bool first = k == 0 && (ss || half == 0);
    if constexpr (ss) {
      if (!(k & 1)) csb = MFMA16(sel, f[u & 3], first ? zero4 : csb);
      accs = MFMA32(xf[2 * k], f[u & 3], first ? zero16 : accs);
    } else {
      if (!(k & 3)) csb = MFMA16(sel, f[u & 3], first ? zero4 : csb);
      acc[p] = mm(f[u & 3], xf[16 * half + k], first ? zero16 : acc[p]);
    }
    SCHED();
    if constexpr (!ss && half == 1 && k == 13) { csa = csb; cs_put(4 * cg + p); }
    if constexpr (ss && k == 15) { csa = csb; cs_put(8); }
    if constexpr (k == 13 && s < 14) {
      if (s == 10) STAMP(11);
      if (s == 12) STAMP(12);
      RBAR(s + 1, k1_wait(GT, s + 1));
      STAMP(s - 3);
      rd(IC<u + 3>{}); rd(IC<u + 4>{});
    } else if constexpr (k != 12 || s == 14) {
      rd(IC<u + 4>{});
    }
    if constexpr (u == 4) BARRIER();
    if constexpr (GT && u == 110) { BARRIER(); }
    constexpr int t = k1_ival(u);
    if constexpr (t + 3 < 15) {
#pragma unroll
      for (int d = 0; d < 4; ++d)
        if (u == k1_dma_unit(t, d)) issue(IC<t + 3>{}, d);
    }
#pragma unroll
    for (int i = 0; i < K1_NPF; ++i)
      if (u == k1_pf_unit(i)) prefetch(i);
    quantum(uc);
    SCHED();
  });
  STAMP(13);
  if (GT) {
    gate_q(2, 31);
    cq_load(4 * cg + 3, 0);
#pragma unroll
    for (int k = 0; k < 8; ++k) {
      up_q(3, k);
      if (k == 3) up_st(3, 0);
    }
    up_st(3, 1);
  } else {
    ssm_q(3, 30); ssm_q(3, 31);
    ss_send(0); ss_send(1);
    BARRIER();
    ssm_pre(8, 0.5f); ss_recv(); ssm_mr(0, 0);
#pragma unroll
    for (int hq = 0; hq < 16; ++hq) ss_q(hq);
    ssm_st(8, 0);
    BARRIER();
    const int ts = 2 * w + (lane >> 5);
    if (ts < 9) {
      pfx_load(ts);
#pragma unroll
      for (int k = 0; k < 19; ++k) pfx_step(k);
      pfx_store(ts);
    }
  }
  STAMP(14);
  VMCNT(0);
  RT1(0, (pi * 16 + nb) * 8 + w, ws);
}

__global__ __launch_bounds__(512, 2) NOPK void k1(K1Args A) {
  extern __shared__ __attribute__((aligned(16))) char smem[];
  const int b = blockIdx.x;
  const int xcd = b & 7, r = b >> 3;
  const int pl = r & 7, nl = r >> 3;
  const int pi = (xcd & 1) * 8 + pl, nb = (xcd >> 1) * 4 + nl;
  const int c0 = (pl + 2 * nl) & 7;
  unsigned long long cpc;
  unsigned clen;
  asm volatile("s_getpc_b64 %0\nk1_pc_ref:\n\ts_mov_b32 %1, k2_code_end-k1_pc_ref" : "=s"(cpc), "=s"(clen));
  clen = clen < 65536u ? clen : 0u;
  if (nb < 14) k1_body<true>(A, smem, pi, nb, c0, r, cpc, clen);
  else k1_body<false>(A, smem, pi, nb, c0, r, cpc, clen);
}

struct K2Args {
  const u16 *x, *Dk, *wd, *Cre, *Cim;
  char* ws;
  float* out;
};
constexpr int L2_P = 131072;
constexpr int L2_EX = 98304;
constexpr int L2_SIZE = L2_P + 32768;
constexpr bool k2_isy(int s) { return s >= 7 && (s & 1); }
constexpr int k2_idx(int s) { return s < 7 ? s : ((s & 1) ? (s - 7) / 2 : (s + 6) / 2); }

__device__ __forceinline__ void k2_load(const K2Args& A, unsigned lds0, int w, int ml, int mi, int nj) {
  const int lane = threadIdx.x & 63, tid = threadIdx.x & 511, pi = mi >> 1;
  char* ws = A.ws;
  const int r8 = lane >> 3, pc = lane & 7;
#define ROT 1
  const int rotg = (7 * ml) >> 1, roty = 2 * ml;
  const unsigned va = w * 2048 + lane * 16;
  const unsigned vw = (16 * w + r8) * 1024 + 16 * (pc ^ (((r8 >> 1) & 1) << 2));
  const unsigned vc = (8 * w + r8) * 1024 + 16 * (pc ^ ((4 * (w & 1) + (r8 >> 1)) & 7));
  auto issue = [&](auto sc) AI {
    constexpr int s = decltype(sc)::value;
    const unsigned sl = lds0 + (s & 3) * 32768;
    if constexpr (s >= 22) {
    } else if constexpr (!k2_isy(s)) {
      const int gs = k2_idx(s) + rotg >= 14 ? k2_idx(s) + rotg - 14 : k2_idx(s) + rotg;
      const char* ab = ws + OFF_A + (size_t)(mi * 14 + gs) * 16384;
      const char* wb = (const char*)A.wd + (size_t)gs * 131072 + nj * 128;
      dmar<s>(ab, va, sl + w * 2048);
      dmar<s>(ab + 1024, va, sl + w * 2048 + 1024);
      dmar<s>(wb, vw, sl + 16384 + w * 2048);
      dmar<s>(wb + 8192, vw, sl + 16384 + w * 2048 + 1024);
    } else {
      const int ys = (k2_idx(s) + roty) & 7;
      const char* hb = ws + OFF_H + (size_t)(mi * 8 + ys) * 16384;
      dmar<s>(hb, va, sl + w * 2048);
      dmar<s>(hb + 1024, va, sl + w * 2048 + 1024);
      dmar<s>((const char*)A.Cre + nj * 65536 + ys * 128, vc, sl + 16384 + w * 1024);
      dmar<s>((const char*)A.Cim + nj * 65536 + ys * 128, vc, sl + 24576 + w * 1024);
    }
  };
  issue(IC<0>{}); issue(IC<1>{}); issue(IC<2>{}); issue(IC<3>{});
  const int part = lane & 1, cl = (lane >> 1) & 7, sg = lane >> 4;
  f32x2 tot[15], a1, at[8];
  f32x4 car[4];
  const unsigned v8 = tid * 8, v16 = tid * 16, vat = ((8 * w + cl) * 2 + part) * 8;
  const unsigned pwa = lds0 + L2_P + (w >> 1) * 4096 + sg * 512 + (((16 * (w & 1) + 2 * cl + part) ^ (sg & 1) ^ ((sg >> 1) << 3)) * 16);
  f32x2 ev[8];
  RBAR(0, 12);
  sfor<0, 21>([&](auto tc) AI {
    constexpr int t = decltype(tc)::value;
    if constexpr (t >= 1) issue(IC<t + 3>{});
    if constexpr (t == 0) {
#pragma unroll
      for (int m = 14; m >= 0; --m) {
        const int i = pi - 1 - m;
        const char* sb = ws + OFF_TOT + (size_t)(i < 0 ? 0 : i) * 4096;
        GLD8S(tot[m], v8, sb);
      }
      const char* sa = ws + OFF_AT + 8;
      GLD8S(a1, v16, sa);
    }
    if constexpr (t == 0) {
#pragma unroll
      for (int g = 0; g < 4; ++g) {
        const char* sb = ws + OFF_CAR + (size_t)((pi * 4 + 2 * (mi & 1) + (g & 1)) * 2 + (g >> 1)) * 8192;
        GLD16S(car[g], v16, sb);
      }
    }
    if constexpr (t == 1) {
#pragma unroll
      for (int j = 0; j < 8; ++j) {
        const char* sb = ws + OFF_AT + ((j + roty) & 7) * 1024;
        GLD8S(at[j], vat, sb);
      }
    }
    if constexpr (t == 2) {
      asm volatile("s_waitcnt vmcnt(16)"
                   : "+v"(tot[0]), "+v"(tot[1]), "+v"(tot[2]), "+v"(tot[3]), "+v"(tot[4]), "+v"(tot[5]), "+v"(tot[6]), "+v"(tot[7]),
                     "+v"(tot[8]), "+v"(tot[9]), "+v"(tot[10]), "+v"(tot[11]), "+v"(tot[12]), "+v"(tot[13]), "+v"(tot[14]), "+v"(a1),
                     "+v"(car[0]), "+v"(car[1]), "+v"(car[2]), "+v"(car[3])::"memory");
      const f32x2 a8 = csq(csq(csq(a1)));
      const f32x2 a64 = csq(csq(csq(a8)));
      const f32x2 aP = csq(a64);
      f32x2 Hc = (mi & 1) ? cmul(a64, a1) : a1;
      tot[14] = pi > 14 ? tot[14] : (f32x2){0.f, 0.f};
#pragma unroll
      for (int m = 13; m >= 0; --m) tot[m] = cfma(aP, tot[m + 1], pi > m ? tot[m] : (f32x2){0.f, 0.f});
      Hc = cmul(Hc, tot[0]);
#pragma unroll
      for (int g = 0; g < 8; ++g) {
        const f32x2 e = cfma(a1, (f32x2){car[g >> 2][g & 3], car[2 + (g >> 2)][g & 3]}, Hc);
        car[g >> 2][g & 3] = e[0];
        car[2 + (g >> 2)][g & 3] = e[1];
        if (g < 7) Hc = cmul(Hc, a8);
      }
#pragma unroll
      for (int g = 0; g < 4; ++g) {
        const int c = g & 2, o = 2 * (g & 1);
        const f32x4 e4 = {car[c][o], car[c + 1][o], car[c][o + 1], car[c + 1][o + 1]};
        LDSW(lds0 + L2_P + tid * 64 + ((g ^ ((tid >> 1) & 3)) * 16), 0, e4);
      }
    }
    if constexpr (t == 4) {
#pragma unroll
      for (int j = 0; j < 8; ++j)
        ev[j] = *LP(f32x2, lds0 + L2_P + (((j + roty) & 7) * 64 + 8 * w + cl) * 64 + (((2 * part + (sg >> 1)) ^ (cl >> 1)) * 16) + (sg & 1) * 8);
      asm volatile("" : "+v"(ev[0]), "+v"(ev[1]), "+v"(ev[2]), "+v"(ev[3]), "+v"(ev[4]), "+v"(ev[5]), "+v"(ev[6]), "+v"(ev[7]));
    }
    if constexpr (t == 5)
      asm volatile("" : "+v"(at[0]), "+v"(at[1]), "+v"(at[2]), "+v"(at[3]), "+v"(at[4]), "+v"(at[5]), "+v"(at[6]), "+v"(at[7])::"memory");
    if constexpr (t == 5 || (t >= 6 && t <= 18 && !(t & 1))) {
      constexpr int j = t == 5 ? 0 : k2_idx(t + 3);
      float p[2] = {ev[j][0], ev[j][1]};
      u32x4 o[2];
#pragma unroll
      for (int q = 0; q < 4; ++q) {
#pragma unroll
        for (int c = 0; c < 2; ++c) {
          const float p1 = fmaf(at[j][0], p[c], SWP(p[c]) * at[j][1]);
          o[c][q] = pk2(p[c], p1);
          p[c] = p1;
        }
        if (q < 3) {
#pragma unroll
          for (int c = 0; c < 2; ++c) p[c] = fmaf(at[j][0], p[c], SWP(p[c]) * at[j][1]);
        }
      }
      LDSW(pwa, (j & 1) * 16384, o[0]);
      LDSW(pwa, (j & 1) * 16384 + 2048, o[1]);
    }
    constexpr int nw = (t + 2 < 22 ? 4 : 0) + (t + 3 < 22 ? 4 : 0) + (t < 3 ? 20 : 0) + (t >= 1 && t < 4 ? 8 : 0);
    constexpr int nl = (t == 3 || t == 4 || t == 20) ? 0 : (k2_isy(t + 1) ? 2 : 15);
    asm volatile("s_waitcnt vmcnt(%0) lgkmcnt(%2) ; NEED %1" ::"i"(nw), "i"(t + 1), "i"(nl) : "memory");
    __builtin_amdgcn_s_barrier();
    asm volatile("" ::: "memory");
  });
}

__device__ __forceinline__ void k2_mul(const K2Args& A, unsigned lds0, int w, int mi, int nj) {
  const int lane = threadIdx.x & 63;
  char* ws = A.ws;
  STAMP(0);
  RT0();
  const int kg = w >> 2, wr = w & 1, wc = (w >> 1) & 1;
  const int n = lane & 31, h = lane >> 5, t16 = lane & 15, g1 = (lane >> 4) & 1;
  const int orow = 64 * mi + 32 * wr + n;
  const int ocol = 64 * nj + 32 * wc + 16 * kg + 4 * h;
  float sgv[8], xfv[8];
  {
    const u32x2 x0 = *(const u32x2*)(A.x + (size_t)orow * D_ + ocol), x1 = *(const u32x2*)(A.x + (size_t)orow * D_ + ocol + 8);
    const u32x2 d0 = *(const u32x2*)(A.Dk + ocol), d1 = *(const u32x2*)(A.Dk + ocol + 8);
    const f32x2 st = *(const f32x2*)(ws + OFF_ST + (size_t)orow * 8);
#pragma unroll
    for (int v = 0; v < 8; ++v) {
      const unsigned xw = (v & 4) ? x1[(v >> 1) & 1] : x0[(v >> 1) & 1], dw = (v & 4) ? d1[(v >> 1) & 1] : d0[(v >> 1) & 1];
      const float xx = bf2f((xw >> (16 * (v & 1))) & 0xffff), dd = bf2f((dw >> (16 * (v & 1))) & 0xffff);
      const float xn = rbf(rbf(xx - st[0]) / st[1]);
      xfv[v] = xx;
      sgv[v] = rbf(xn * dd);
      asm volatile("" : "+v"(xfv[v]), "+v"(sgv[v]));
    }
  }
  STAMP(1);
  const unsigned arb = lds0 + (8 * kg + wr) * 1024 + lane * 16;
  const unsigned trw = lds0 + 16384 + (64 * kg + 4 * h + (t16 >> 2)) * 128 + 32 * ((2 * wc + (g1 ^ kg)) ^ ((t16 >> 3) << 1)) + 8 * (t16 & 3);
  const unsigned trh0 = lds0 + (4 * wr + 2 * g1 + ((t16 >> 1) & 1)) * 512 + ((16 * h + 8 * g1 + 2 * (t16 >> 2) + (kg ^ ((t16 >> 1) & 1))) * 16) + (t16 & 1) * 8;
  const unsigned trh1 = trh0 ^ 128;
  const int cd = 32 * wc + (n ^ (16 * kg));
  const unsigned crb = lds0 + 16384 + kg * 8192 + cd * 128 + 16 * (h ^ ((cd >> 1) & 7));
  f32x16 accg, accy;
#pragma unroll
  for (int i = 0; i < 16; ++i) { accg[i] = 0.f; accy[i] = 0.f; }
  f32x4 gp[2];
  bf16x8 fa[4], fb[4], fp[4];
  unsigned ga[2] = {arb, arb + 65536}, gb[2] = {trw, trw + 65536};
  unsigned ya[3] = {trh0, trh0 + 65536, trh0 + 131072}, yb[3] = {trh1, trh1 + 65536, trh1 + 131072}, yc[2][4];
#pragma unroll
  for (int i = 0; i < 4; ++i) { yc[0][i] = crb ^ (i << 5); yc[1][i] = yc[0][i] + 65536; }
  asm volatile("" : "+v"(ga[1]), "+v"(gb[1]), "+v"(ya[1]), "+v"(ya[2]), "+v"(yb[1]), "+v"(yb[2]));
  asm volatile("" : "+v"(yc[1][0]), "+v"(yc[1][1]), "+v"(yc[1][2]), "+v"(yc[1][3]));
  auto rd = [&](auto uc) AI {
    constexpr int u = decltype(uc)::value, s = u >> 2, i = u & 3, so = (s & 1) * 32768, hf = (s >> 1) & 1;
    if constexpr (u >= 88) {
    } else if constexpr (!k2_isy(s)) {
      fa[i] = *LP(bf16x8, ga[hf] + so + i * 2048);
      fb[i] = trfrag(gb[hf] + so + i * 2048, 1024);
    } else {
      fa[i] = trfrag2(ya[hf] + so + i * 4096, yb[hf] + so + i * 4096);
      fb[i] = *LP(bf16x8, yc[hf][i] + so);
      constexpr int po = (k2_idx(s) & 1) * 16384 + i * 4096;
      fp[i] = trfrag2(ya[2] + po, yb[2] + po);
    }
  };
  BARRIER();
  STAMP(2);
  rd(IC<0>{}); rd(IC<1>{});
  SCHED();
  sfor<0, 88>([&](auto uc) AI {
    constexpr int u = decltype(uc)::value, s = u >> 2, i = u & 3;
    constexpr bool isy = k2_isy(s);
    if constexpr (!isy) {
      accg = MFMA32(fb[i], fa[i], accg);
    } else {
      accy = MFMA32(fb[i], fa[i], accy);
      accy = MFMA32(fb[i], fp[i], accy);
    }
    SCHED();
    if constexpr (i == 2) BARRIER();
    rd(IC<u + 2>{});
    if constexpr (u == 84) {
#pragma unroll
      for (int e = 0; e < 2; ++e)
        *LP(f32x4, lds0 + L2_EX + (w * 2 + e) * 1024 + lane * 16) =
            (f32x4){accg[8 + 4 * e], accg[9 + 4 * e], accg[10 + 4 * e], accg[11 + 4 * e]};
    }
    if constexpr (u == 86) {
#pragma unroll
      for (int e = 0; e < 2; ++e) gp[e] = *LP(f32x4, lds0 + L2_EX + ((w ^ 4) * 2 + e) * 1024 + lane * 16);
    }
    if constexpr (u >= 86) {
#pragma unroll
      for (int v = 4 * (u - 86); v < 4 * (u - 86) + 4; ++v) {
        const float g = rbf(accg[v] + gp[v >> 2][v & 3]);
        const float sv = rbf(g * __builtin_amdgcn_rcpf(1.f + __expf(-g)));
        xfv[v] = fmaf(sgv[v], sv, xfv[v]);
        sgv[v] = 2.f * sv;
      }
    }
    SCHED();
  });
#pragma unroll
  for (int e = 0; e < 2; ++e)
    *LP(f32x4, lds0 + 65536 + (w * 2 + e) * 1024 + lane * 16) =
        (f32x4){accy[8 + 4 * e], accy[9 + 4 * e], accy[10 + 4 * e], accy[11 + 4 * e]};
  BARRIER();
#pragma unroll
  for (int e = 0; e < 2; ++e) {
    const f32x4 pq = *LP(f32x4, lds0 + 65536 + ((w ^ 4) * 2 + e) * 1024 + lane * 16);
    f32x4 o;
#pragma unroll
    for (int rr = 0; rr < 4; ++rr) o[rr] = fmaf(accy[4 * e + rr] + pq[rr], sgv[4 * e + rr], xfv[4 * e + rr]);
    *(f32x4*)(A.out + (size_t)orow * D_ + ocol + 8 * e) = o;
  }
  STAMP(11);
  RT1(1, blockIdx.x * 8 + w, ws);
}

__global__ __launch_bounds__(1024, 4) NOPK void k2(K2Args A) {
  extern __shared__ __attribute__((aligned(16))) char smem[];
  const unsigned lds0 = (unsigned)(uintptr_t)(LAS char*)smem;
  const int w = __builtin_amdgcn_readfirstlane(threadIdx.x >> 6) & 15;
  const int b = blockIdx.x;
  const int xcd = b & 7, r = b >> 3;
  const int ml = r >> 3, mi = xcd * 4 + ml, nj = r & 7;
  if (w >= 8) {
    k2_load(A, lds0, w & 7, ml, mi, nj);
  } else {
    __builtin_amdgcn_s_setprio(1);
    k2_mul(A, lds0, w, mi, nj);
  }
  asm volatile("k2_code_end:");
}

extern "C" void kernel_launch(void* const* d_in, const int* in_sizes, int n_in, void* d_out, int out_size, void* d_ws,
                              size_t ws_size, hipStream_t stream) {
  K1Args a;
  a.x = (const u16*)d_in[0]; a.Lre = (const u16*)d_in[1]; a.Lim = (const u16*)d_in[2];
  a.Bre = (const u16*)d_in[3]; a.Bim = (const u16*)d_in[4]; a.Cre = (const u16*)d_in[5]; a.Cim = (const u16*)d_in[6];
  a.ldt = (const u16*)d_in[7]; a.wg = (const u16*)d_in[10]; a.wu = (const u16*)d_in[11]; a.wd = (const u16*)d_in[12];
  a.ws = (char*)d_ws;
  (void)hipFuncSetAttribute((const void*)k1, hipFuncAttributeMaxDynamicSharedMemorySize, L1_SIZE);
  (void)hipFuncSetAttribute((const void*)k2, hipFuncAttributeMaxDynamicSharedMemorySize, L2_SIZE);
  k1<<<256, 512, L1_SIZE, stream>>>(a);
  K2Args b;
  b.x = (const u16*)d_in[0]; b.Dk = (const u16*)d_in[8]; b.wd = (const u16*)d_in[12];
  b.Cre = (const u16*)d_in[5]; b.Cim = (const u16*)d_in[6];
  b.ws = (char*)d_ws; b.out = (float*)d_out;
  k2<<<256, 1024, L2_SIZE, stream>>>(b);
}
```

```cpp
#include <hip/hip_runtime.h>
#include <hip/hip_bf16.h>
#include <stdint.h>

typedef unsigned short u16;
typedef __attribute__((ext_vector_type(8))) short bf16x8;
typedef __attribute__((ext_vector_type(4))) short s16x4;
typedef __attribute__((ext_vector_type(16))) float f32x16;
typedef __attribute__((ext_vector_type(4))) float f32x4;
typedef __attribute__((ext_vector_type(2))) float f32x2;
typedef __attribute__((ext_vector_type(4))) unsigned u32x4;
typedef __attribute__((ext_vector_type(2))) unsigned u32x2;
typedef __attribute__((ext_vector_type(2))) __bf16 bf16x2_t;
#define LAS __attribute__((address_space(3)))
#if defined(__HIP_DEVICE_COMPILE__)
#define NOPK __attribute__((target("no-packed-fp32-ops")))
#else
#define NOPK
#endif

constexpr int D_ = 512;

constexpr size_t OFF_A   = 0;
constexpr size_t OFF_H   = OFF_A + (size_t)32 * 112 * 2048;
constexpr size_t OFF_CAR = OFF_H + (size_t)32 * 32 * 4096;
constexpr size_t OFF_TOT = OFF_CAR + (size_t)16 * 1024 * 64;
constexpr size_t OFF_AT  = OFF_TOT + (size_t)16 * 4096;
constexpr size_t OFF_ST  = OFF_AT + 8192;
constexpr size_t OFF_CNT = OFF_ST + 16384;
constexpr size_t OFF_HF  = OFF_CNT + 4096;

constexpr int L1_AGG = 131072;
constexpr int L1_AC  = L1_AGG + 18432;
constexpr int L1_STF = L1_AC + 4608;
constexpr int L1_ST  = L1_STF + 2048;
constexpr int L1_CS  = L1_ST + 2048;
constexpr int L1_CSR = L1_AC;
constexpr int L1_DUM = L1_CS + 4608;
constexpr int L1_SIZE = L1_DUM + 1024;

__device__ __forceinline__ float bf2f(unsigned h) { return __builtin_bit_cast(float, h << 16); }
__device__ __forceinline__ float rbf(float v) { return (float)(__bf16)v; }
__device__ __forceinline__ unsigned pk2(float lo, float hi) {
  bf16x2_t r = {(__bf16)lo, (__bf16)hi};
  return __builtin_bit_cast(unsigned, r);
}
template <int CTRL> __device__ __forceinline__ float dpp0(float x) {
  return __builtin_bit_cast(float, __builtin_amdgcn_update_dpp(0, __builtin_bit_cast(int, x), CTRL, 0xf, 0xf, true));
}
#define SWP(x) dpp0<0xB1>(x)
__device__ __forceinline__ f32x2 csq(f32x2 a) { return (f32x2){fmaf(a[0], a[0], -a[1] * a[1]), 2.f * a[0] * a[1]}; }
__device__ __forceinline__ f32x2 cmul(f32x2 a, f32x2 b) {
  return (f32x2){fmaf(a[0], b[0], -a[1] * b[1]), fmaf(a[0], b[1], a[1] * b[0])};
}
__device__ __forceinline__ f32x2 cfma(f32x2 a, f32x2 b, f32x2 c) {
  return (f32x2){fmaf(a[0], b[0], fmaf(-a[1], b[1], c[0])), fmaf(a[0], b[1], fmaf(a[1], b[0], c[1]))};
}
template <int V> struct IC { static constexpr int value = V; };
template <int I, int N, class F> __device__ __forceinline__ void sfor(F&& f) {
  if constexpr (I < N) { f(IC<I>{}); sfor<I + 1, N>(f); }
}
__device__ __forceinline__ const char* uptr(const void* base, unsigned off) {
  return (const char*)base + (size_t)(unsigned)__builtin_amdgcn_readfirstlane(off);
}
#define MFMA16(a, b, c) __builtin_amdgcn_mfma_f32_16x16x32_bf16(a, b, c, 0, 0, 0)
#define MFMA32(a, b, c) __builtin_amdgcn_mfma_f32_32x32x16_bf16(a, b, c, 0, 0, 0)
#define SCHED() __builtin_amdgcn_sched_barrier(0)
#define LP(T, a) ((LAS T*)(unsigned)(a))
#define AI __attribute__((always_inline))
#define LMASK(m) __builtin_amdgcn_inverse_ballot_w64(m)
#define LB0 LMASK(0xAAAAAAAAAAAAAAAAull)
#define LB1 LMASK(0xCCCCCCCCCCCCCCCCull)
#define LB3 LMASK(0xFF00FF00FF00FF00ull)
#define LB4 LMASK(0xFFFF0000FFFF0000ull)
#define LLO LMASK(0x00000000FFFFFFFFull)

#define GLD16(dst, p) asm volatile("global_load_dwordx4 %0, %1, off" : "=v"(dst) : "v"(p) : "memory")
#define GLD8(dst, p) asm volatile("global_load_dwordx2 %0, %1, off" : "=v"(dst) : "v"(p) : "memory")
#define GLD2(dst, p) asm volatile("global_load_ushort %0, %1, off" : "=v"(dst) : "v"(p) : "memory")
#define GLD16S(dst, vo, sb) asm volatile("global_load_dwordx4 %0, %1, %2" : "=v"(dst) : "v"(vo), "s"(sb) : "memory")
#define GLD8S(dst, vo, sb) asm volatile("global_load_dwordx2 %0, %1, %2" : "=v"(dst) : "v"(vo), "s"(sb) : "memory")
#define GST16S(sb, vo, v) asm volatile("global_store_dwordx4 %0, %1, %2 sc0 sc1\n\ts_nop 1" ::"v"(vo), "v"(v), "s"(sb) : "memory")
#define LDSW(a, off, v) asm volatile("ds_write_b128 %0, %1 offset:%2" ::"v"(a), "v"(v), "i"(off) : "memory")
#define VMCNT(n) asm volatile("s_waitcnt vmcnt(%0)" ::"i"(n) : "memory")
#define BARRIER() do { asm volatile("s_waitcnt lgkmcnt(0)" ::: "memory"); __builtin_amdgcn_s_barrier(); asm volatile("" ::: "memory"); } while (0)
#define RBAR(s, n) do { asm volatile("s_waitcnt vmcnt(%0) lgkmcnt(0) ; NEED %1" ::"i"(n), "i"(s) : "memory"); __builtin_amdgcn_s_barrier(); asm volatile("" ::: "memory"); } while (0)
__device__ __forceinline__ void dma(const char* sbase, unsigned voff, unsigned lds_dst) {
  asm volatile("s_mov_b32 m0, %2\n\ts_nop 0\n\tglobal_load_lds_dwordx4 %0, %1" ::"v"(voff), "s"(sbase), "s"(lds_dst) : "memory");
}
template <int S> __device__ __forceinline__ void dmar(const char* sbase, unsigned voff, unsigned lds_dst) {
  asm volatile("s_mov_b32 m0, %2\n\ts_nop 0\n\tglobal_load_lds_dwordx4 %0, %1 ; RING %3" ::"v"(voff), "s"(sbase), "s"(lds_dst), "i"(S) : "memory");
}
__device__ __forceinline__ bf16x8 trfrag2(unsigned a, unsigned b) {
  s16x4 lo = __builtin_amdgcn_ds_read_tr16_b64_v4i16(LP(s16x4, a));
  s16x4 hi = __builtin_amdgcn_ds_read_tr16_b64_v4i16(LP(s16x4, b));
  return __builtin_shufflevector(lo, hi, 0, 1, 2, 3, 4, 5, 6, 7);
}
__device__ __forceinline__ bf16x8 trfrag(unsigned a, int d) {
  s16x4 lo = __builtin_amdgcn_ds_read_tr16_b64_v4i16(LP(s16x4, a));
  s16x4 hi = __builtin_amdgcn_ds_read_tr16_b64_v4i16(LP(s16x4, a + d));
  return __builtin_shufflevector(lo, hi, 0, 1, 2, 3, 4, 5, 6, 7);
}

#define STAMP(i)
constexpr size_t OFF_DBG = (size_t)64 << 20;
#define RT0()
#define RT1(a,b,c)

struct K1Args {
  const u16 *x, *Lre, *Lim, *Bre, *Bim, *Cre, *Cim, *ldt, *wg, *wu, *wd;
  char* ws;
};

constexpr int k1_bar_unit(int s) { return 16 * (s - 8) - 3; }
constexpr int k1_ival(int u) { return 8 + (u + 3) / 16; }
constexpr int k1_nring(int s) { return s > 14 ? 0 : (s < 8 ? 3 : 4); }
constexpr int k1_dma_unit(int t, int d) { return t == 8 ? (d == 0 ? 1 : 4 * d - 2) : k1_bar_unit(t) + 1 + 4 * d; }
#define PFS 128
constexpr int K1_NPF = (90112 / (64 * PFS) + 7) / 8;
constexpr int k1_pf_unit(int n) { return 64 + 4 * n; }
constexpr int k1_s_unit(int p, int hq) { return p == 0 ? 10 + (2 * hq) / 3 : (p == 3 ? 97 + hq / 2 : 32 * p + 1 + (2 * hq) / 3); }
constexpr int k1_extra(bool gt, int u) {
  int n = 0;
  for (int i = 0; i < K1_NPF; ++i) n += k1_pf_unit(i) == u;
  if (gt) n += (u == 53) + (u == 57) + (u == 75);
  else
    for (int p = 0; p < 3; ++p) n += (k1_s_unit(p, 29) == u) + (k1_s_unit(p, 31) == u);
  return n;
}
constexpr int k1_wait(bool gt, int s) {
  int n = k1_nring(s + 1) + k1_nring(s + 2);
  for (int u = s >= 11 ? k1_dma_unit(s - 3, 3) : 0; u < k1_bar_unit(s); ++u) n += k1_extra(gt, u);
  return n;
}

template <bool GT>
__device__ __forceinline__ void k1_body(const K1Args& A, char* smem, int pi, int nb, int c0, int rx, unsigned long long cpc,
                                        unsigned clen) {
  const int tid = threadIdx.x, lane = tid & 63;
  const int w = __builtin_amdgcn_readfirstlane(tid >> 6) & 7;
  const int rg = w & 3, cg = w >> 2, X = rg ^ cg;
  STAMP(0);
  RT0();
  const unsigned lds0 = (unsigned)(uintptr_t)(LAS char*)smem;
  const int r8 = lane >> 3, pc = lane & 7;
  const int tb0 = GT ? nb - 8 : 14 + 9 * (nb - 14);
  constexpr int SSLOT = GT ? 8 : 14;
  auto olane = [&]() AI -> int { int l = lane; asm volatile("" : "+v"(l)); return l; };

  const char* xb = uptr(A.x, pi * 131072 + w * 16384);
  const unsigned vs = r8 * 1024 + 16 * (pc ^ ((r8 >> 1) | ((w & 1) << 2)));
  const unsigned vw = GT ? (8 * w + r8) * 3584 + 16 * (pc ^ (((r8 >> 1) & 1) << 2)) : vs;
  const char* bmat = uptr((w & 1) ? A.Bim : A.Bre, ((GT ? nb : tb0) * 16 + 8 * ((w >> 1) & 1)) * 1024);
  auto wbase = [&](int p, int c) AI -> const char* {
    if (GT) return (const char*)((p & 1) ? A.wu : A.wg) + (size_t)c * (64 * 3584) + (128 * nb + 64 * (p >> 1)) * 2;
    return bmat + (unsigned)((4 * cg + p) * 16384 + c * 128);
  };
  auto issue = [&](auto sc, int d) AI {
    constexpr int s = decltype(sc)::value;
    const unsigned dst = lds0 + (s & 3) * 32768;
    if constexpr (s < 8) {
      const int c = (c0 + s) & 7;
      if (d == 0) dmar<s>(xb + c * 128, vs, dst + w * 2048);
      if (d == 1) dmar<s>(wbase(0, c), vw, dst + 16384 + w * 1024);
      if (d == 2) dmar<s>(xb + c * 128 + 8192, vs, dst + w * 2048 + 1024);
    } else if constexpr (s == SSLOT) {
      const int i = 4 * cg + d;
      dmar<s>(uptr(bmat, (GT ? 0 : 8 * 16384) + ((c0 + i) & 7) * 128), vs, dst + i * 4096 + rg * 1024);
    } else if constexpr (s < 15) {
      constexpr int sp = GT ? s - 9 : s - 8;
      dmar<s>(wbase(1 + (sp >> 1), (c0 + 4 * (sp & 1) + d) & 7), vw, dst + d * 8192 + w * 1024);
    }
  };
  auto prefetch = [&](int n) AI {
    const unsigned o0 = (n * 8 + w) * (64 * PFS);
    const unsigned o = o0 < 90112 ? o0 : 0, stride = o0 < 90112 ? PFS : 0;
    const uint64_t b0 = (uint64_t)A.wd + rx * 57344, b1 = (uint64_t)A.Cre + rx * 16384 - 57344, b2 = (uint64_t)A.Cim + rx * 16384 - 73728;
    const uint64_t b12 = o < 73728 ? b1 : b2;
    const uint64_t sb = (o < 57344 ? b0 : b12) + o;
    const unsigned vo = olane() * stride;
    asm volatile("s_mov_b32 m0, %2\n\ts_nop 0\n\tglobal_load_lds_dword %0, %1" ::"v"(vo), "s"(sb), "s"(lds0 + L1_DUM) : "memory");
  };

  {
    unsigned off = ((rx & 1) * 8 + w) * 4096 + lane * 64;
    off = off < clen ? off : 0;
    asm volatile("s_mov_b32 m0, %2\n\ts_nop 0\n\tglobal_load_lds_dword %0, %1" ::"v"(off), "s"(cpc), "s"(lds0 + L1_DUM) : "memory");
  }
  unsigned plr, pli, pdt;
  const int tsp = GT ? 8 : ((tid >> 5) < 8 ? (tid >> 5) : 8);
  {
    const int ch = 16 * (tb0 + tsp) + ((tid & 31) >> 1);
    GLD2(plr, A.Lre + ch); GLD2(pli, A.Lim + ch); GLD2(pdt, A.ldt + ch);
  }
  issue(IC<0>{}, 0); issue(IC<0>{}, 1); issue(IC<0>{}, 2);
  __builtin_amdgcn_s_barrier();
  issue(IC<1>{}, 0); issue(IC<1>{}, 1); issue(IC<1>{}, 2);
  __builtin_amdgcn_s_barrier();
  issue(IC<2>{}, 0); issue(IC<2>{}, 1); issue(IC<2>{}, 2);
  STAMP(1);
  char* ws = A.ws;
  {
    asm volatile("s_waitcnt vmcnt(9)" : "+v"(plr), "+v"(pli), "+v"(pdt)::"memory");
    const float lr = bf2f(plr), li = bf2f(pli);
    const float dt = rbf(expf(bf2f(pdt)));
    float sn, cs;
    sincosf(dt * li, &sn, &cs);
    const float e = expf(dt * lr);
    const float ar = e * cs, ai = e * sn;
    const float nr = ar - 1.f, den = lr * lr + li * li;
    const float fr = (nr * lr + ai * li) / den, fi = (ai * lr - nr * li) / den;
    const bool im = tid & 1;
    const f32x4 ac = {ar, im ? -ai : ai, im ? -fr : fr, -fi};
    *LP(f32x4, lds0 + L1_AC + (tsp * 32 + (tid & 31)) * 16) = ac;
    if (pi == 0 && tid < (GT ? 32 : 288))
      *(f32x2*)(ws + OFF_AT + (size_t)((tb0 + tsp) * 32 + (tid & 31)) * 8) = (f32x2){ac[0], ac[1]};
    if (pi == 1 && nb == 0 && tid < 32) *(unsigned*)(ws + OFF_CNT + tid * 128) = 0u;
  }

  const int n = lane & 31, h = lane >> 5, t16 = lane & 15;
  const int rho = 16 * ((n >> 2) & 1) + 4 * (n >> 3) + (n & 3);
  const int tau = rho ^ (8 * cg);
  const unsigned selw = (t16 == 4 * ((lane >> 4) & 1)) ? 0x3F803F80u : 0u;
  const bf16x8 sel = __builtin_bit_cast(bf16x8, (u32x4){selw, selw, selw, selw});
  const unsigned axb = lds0 + (32 * rg + tau) * 128 + 16 * ((2 * X + h) ^ (((tau >> 1) & 3) | ((tau >> 4) << 2)));
  auto sbase = [&](int l) AI -> unsigned {
    const int nn = l & 31, hh = l >> 5;
    return lds0 + (16 * (nn >> 4) + 8 * (nn & 1) + ((nn >> 1) & 7)) * 128 + 16 * ((2 * X + hh) ^ (((nn >> 2) & 3) | ((nn & 1) << 2)));
  };
  unsigned aw[4];
  {
    const unsigned b = GT ? lds0 + (16 * X + 8 * h + (t16 >> 2)) * 128 + 32 * ((2 * cg + ((lane >> 4) & 1)) ^ ((t16 >> 3) << 1)) + 8 * (t16 & 3)
                          : sbase(lane) + cg * 4096;
#pragma unroll
    for (int j = 0; j < 4; ++j) aw[j] = b ^ (GT ? j << 11 : j << 5);
  }
  auto flip = [&]() AI {
#pragma unroll
    for (int j = 0; j < 4; ++j) aw[j] ^= 65536;
    asm volatile("" : "+v"(aw[0]), "+v"(aw[1]), "+v"(aw[2]), "+v"(aw[3]));
  };
  auto wfrag = [&](unsigned a) AI -> bf16x8 {
    if (GT) return trfrag(a, 512);
    return *LP(bf16x8, a);
  };
  auto mm = [&](bf16x8 wfv, bf16x8 xfv, f32x16 c) AI -> f32x16 { return GT ? MFMA32(wfv, xfv, c) : MFMA32(xfv, wfv, c); };

  const f32x4 zero4 = {0.f, 0.f, 0.f, 0.f};
  f32x16 zero16;
#pragma unroll
  for (int i = 0; i < 16; ++i) zero16[i] = 0.f;
  f32x16 acc[4], accs, gr = zero16;
  f32x4 sm = zero4, csa = zero4;
  acc[0] = zero16;
  bf16x8 xf[32];
  bf16x8 wd2, wd3;
  auto p1tail = [&](int i) AI {
    gr = MFMA32(xf[4 * i + 2], xf[4 * i + 2], gr);
    sm = MFMA16(sel, xf[4 * i + 2], sm);
    acc[0] = mm(wd2, xf[4 * i + 2], acc[0]);
    acc[0] = mm(wd3, xf[4 * i + 3], acc[0]);
  };
  sfor<0, 8>([&](auto ic) AI {
    constexpr int i = decltype(ic)::value;
    RBAR(i, k1_wait(GT, i));
    if (i == 0) STAMP(2);
    if (i == 4) STAMP(3);
    constexpr int so = (i & 1) * 32768;
    const unsigned ax = axb + (i & 2) * 32768;
    xf[4 * i] = *LP(bf16x8, ax + so);
    const bf16x8 w0 = wfrag(aw[0] + so + 16384);
    xf[4 * i + 1] = *LP(bf16x8, (ax ^ 32) + so);
    const bf16x8 w1 = wfrag(aw[1] + so + 16384);
    xf[4 * i + 2] = *LP(bf16x8, (ax ^ 64) + so);
    const bf16x8 w2 = wfrag(aw[2] + so + 16384);
    xf[4 * i + 3] = *LP(bf16x8, (ax ^ 96) + so);
    const bf16x8 w3 = wfrag(aw[3] + so + 16384);
    if (i & 1) flip();
    SCHED();
    if (i > 0) { gr = MFMA32(xf[4 * i - 2], xf[4 * i - 2], gr); SCHED(); }
    issue(IC<i + 3>{}, 0);
    SCHED();
    if (i > 0) {
      acc[0] = mm(wd2, xf[4 * i - 2], acc[0]);
      sm = MFMA16(sel, xf[4 * i - 2], sm);
      acc[0] = mm(wd3, xf[4 * i - 1], acc[0]);
      SCHED();
    }
    issue(IC<i + 3>{}, 1);
    SCHED();
    gr = MFMA32(xf[4 * i], xf[4 * i], gr);
    sm = MFMA16(sel, xf[4 * i], sm);
    acc[0] = mm(w0, xf[4 * i], acc[0]);
    SCHED();
    issue(IC<i + 3>{}, 2);
    SCHED();
    csa = MFMA16(sel, w0, csa);
    acc[0] = mm(w1, xf[4 * i + 1], acc[0]);
    SCHED();
    if (i + 3 >= 8) issue(IC<i + 3>{}, 3);
    wd2 = w2; wd3 = w3;
    SCHED();
  });

  float nmb, sdv, rsg;
  float dgt[4];
  f32x2 stp;
  f32x4 cq[2];
  f32x4 mq[2], rq[2], acv;
  float csv, zc[2], yv[2], tg;
  unsigned ob[8];
  f32x4 pv[2], csp, stq;
  auto cs_put = [&](int ts) AI {
    const int l = olane();
    if (LLO) *LP(float, lds0 + L1_CS + ts * 512 + X * 4 + l * 16) = csa[0];
  };
  auto cs_get = [&](int ts) AI { csp = *LP(f32x4, lds0 + L1_CS + ts * 512 + (olane() & 31) * 16); };
  auto cs_red = [&]() AI {
    *LP(float, lds0 + L1_CSR + w * 128 + (olane() & 31) * 4) = (csp[0] + csp[1]) + (csp[2] + csp[3]);
  };
  auto stat_q = [&](int k) AI {
    const int l = olane();
    if (k < 2) {
#pragma unroll
      for (int a = 2 * k; a < 2 * k + 2; ++a) {
        float g0 = gr[4 * a], g1 = gr[4 * a + 1], g2 = gr[4 * a + 2], g3 = gr[4 * a + 3];
        asm volatile("" : "+v"(g0), "+v"(g1), "+v"(g2), "+v"(g3));
        const float lo = LB0 ? g1 : g0, hi = LB0 ? g3 : g2;
        dgt[a] = LB1 ? hi : lo;
        asm volatile("" : "+v"(dgt[a]));
      }
    } else if (k == 2) {
      const float lo = LB3 ? dgt[1] : dgt[0], hi = LB3 ? dgt[3] : dgt[2];
      const float dg = LB4 ? hi : lo;
      const unsigned a = lds0 + L1_ST + (32 * rg + tau) * 16 + cg * 8;
      if (LMASK(0xF0F0F0F00F0F0F0Full)) *LP(float, a) = dg;
      if (LLO) *LP(float, a + 4) = sm[0];
    } else if (k == 3) {
      cs_put(4 * cg);
    } else if (k == 5) {
      stq = *LP(f32x4, lds0 + L1_ST + (32 * rg + tau) * 16);
    } else if (k == 6) {
      const float mean = (stq[1] + stq[3]) * (1.f / 512.f);
      const float var = fmaxf(fmaf(stq[0] + stq[2], 1.f / 512.f, -mean * mean), 0.f);
      stp = (f32x2){rbf(mean), var};
    } else if (k == 7) {
      stp[1] = rbf(rbf(stp[1]) + 1e-5f);
    } else if (k == 8) {
      sdv = rbf(__builtin_sqrtf(stp[1]));
      nmb = -stp[0];
    } else if (k == 9) {
      const float rs = __builtin_amdgcn_rcpf(sdv);
      rsg = rs * -1.44269504088896f;
      *LP(float, lds0 + L1_STF + w * 256 + (l >> 5) * 128 + rho * 4) = (l >> 5) ? rs : nmb;
      if (nb == 0 && cg == 0 && l < 32) *(f32x2*)(ws + OFF_ST + (size_t)(pi * 128 + rg * 32 + tau) * 8) = (f32x2){stp[0], sdv};
    }
  };
  auto cq_load = [&](int ts, int a) AI { cq[a & 1] = *LP(f32x4, lds0 + L1_CSR + w * 128 + a * 32 + (olane() >> 5) * 16); };
  auto gate_q = [&](int p, int hq) AI {
    const int v = hq >> 1;
    if ((hq & 7) == 4 && hq < 24) cq_load(4 * cg + p, (hq >> 3) + 1);
    if (!(hq & 1)) {
      tg = __builtin_amdgcn_exp2f(fmaf(nmb, cq[(v >> 2) & 1][v & 3], acc[p][v]) * rsg);
    } else {
      float sv = __builtin_amdgcn_rcpf(fmaf(tg, sdv, sdv));
      asm volatile("" : "+v"(sv));
      acc[p][v] = sv;
    }
  };
  auto up_q = [&](int p, int k) AI {
    if (!(k & 1) && k < 6) cq_load(4 * cg + p, (k >> 1) + 1);
    const int v = 2 * k;
    const float h0 = fmaf(nmb, cq[(v >> 2) & 1][v & 3], acc[p][v]) * acc[p - 1][v];
    const float h1 = fmaf(nmb, cq[(v >> 2) & 1][(v & 3) + 1], acc[p][v + 1]) * acc[p - 1][v + 1];
    ob[k] = pk2(h0, h1);
  };
  auto up_st = [&](int p, int e) AI {
    const u32x4 o = {ob[4 * e], ob[4 * e + 1], ob[4 * e + 2], ob[4 * e + 3]};
    const char* sb = uptr(ws, (unsigned)OFF_A + (((2 * pi + (rg >> 1)) * 112 + 8 * nb + 4 * (p >> 1) + 2 * cg + e) * 2 + (rg & 1)) * 1024);
    const unsigned vo = (tau + (olane() & 32)) * 16;
    GST16S(sb, vo, o);
  };
  auto ssm_pre = [&](int ts, float scale) AI {
    const int l = olane();
    acv = *LP(f32x4, lds0 + L1_AC + (ts * 32 + (l & 31)) * 16);
    csp = *LP(f32x4, lds0 + L1_CS + ts * 512 + (l & 31) * 16);
    csv = (csp[0] + csp[1]) + (csp[2] + csp[3]);
  };
  auto ssm_mr = [&](int c, int g) AI {
    const unsigned a = lds0 + L1_STF + w * 256 + (olane() >> 5) * 64 + (8 * c + 4 * g) * 4;
    mq[c] = *LP(f32x4, a);
    rq[c] = *LP(f32x4, a + 128);
  };
  auto ssm_a = [&](float av, int c, int i) AI {
    const float t = fmaf(mq[c][i & 3], csv, av);
    yv[c] = fmaf(acv[2], t, SWP(t) * acv[3]);
  };
  auto ssm_b = [&](int c, int i) AI {
    const float zo = zc[c];
    zc[c] = i == 0 ? rq[c][0] * yv[c] : fmaf(rq[c][i & 3], yv[c], fmaf(acv[0], zo, SWP(zo) * acv[1]));
    if (i & 1) ob[4 * c + (i >> 1)] = pk2(zo, zc[c]);
  };
  auto ssm_st = [&](int ts, int c) AI {
    const int l = olane();
    const u32x4 o = {ob[4 * c], ob[4 * c + 1], ob[4 * c + 2], ob[4 * c + 3]};
    const char* sb = uptr(ws, (unsigned)OFF_H + (((2 * pi + (rg >> 1)) * 32 + tb0 + ts) * 8 + 4 * (rg & 1) + (c ^ cg)) * 512);
    const unsigned vo = ((l & 31) ^ (c ^ cg) ^ ((l >> 5) << 3)) * 16 + (l >> 5) * 1024;
    GST16S(sb, vo, o);
    *LP(float, lds0 + L1_AGG + ts * 2048 + (l & 31) * 64 + ((rg ^ ((l >> 2) & 3)) * 16) + (2 * (l >> 5) + (c ^ cg)) * 4) = zc[c];
  };
  auto ssm_q = [&](int p, int hq) AI {
    const int c = (hq >> 1) & 1, i = hq >> 2;
    if (!(hq & 1)) ssm_a(acc[p][8 * c + i], c, i);
    else {
      ssm_b(c, i);
      if (i == 3) ssm_mr(c, 1);
      if (i == 7) ssm_st(4 * cg + p, c);
    }
  };
  const unsigned exs = lds0 + (GT ? L1_AGG : 0);
  auto ss_send = [&](int g) AI {
    *LP(f32x4, exs + w * 2048 + g * 1024 + olane() * 16) = (f32x4){accs[8 + 4 * g], accs[9 + 4 * g], accs[10 + 4 * g], accs[11 + 4 * g]};
  };
  auto ss_recv = [&]() AI {
    const unsigned a = exs + (w ^ 4) * 2048 + olane() * 16;
    pv[0] = *LP(f32x4, a);
    pv[1] = *LP(f32x4, a + 1024);
  };
  auto ss_q = [&](int hq) AI {
    const int i = hq >> 1;
    if (!(hq & 1)) ssm_a(accs[i] + pv[i >> 2][i & 3], 0, i);
    else {
      ssm_b(0, i);
      if (i == 3) ssm_mr(0, 1);
    }
  };
  f32x4 pz[4];
  f32x2 pa;
  float pcv;
  auto pfx_load = [&](int ts) AI {
    const int l = olane();
    pa = *LP(f32x2, lds0 + L1_AC + (ts * 32 + (l & 31)) * 16);
    const unsigned a = lds0 + L1_AGG + ts * 2048 + (l & 31) * 64 + ((l >> 2) & 3) * 16;
#pragma unroll
    for (int g = 0; g < 4; ++g) pz[g] = *LP(f32x4, a ^ (g * 16));
  };
  auto pfx_step = [&](int k) AI {
    if (k < 3) pa = csq(pa);
    else {
      const int s = k - 3;
      const float z = pz[s >> 2][s & 3];
      pz[s >> 2][s & 3] = s == 0 ? 0.f : pcv;
      pcv = s == 0 ? z : fmaf(pa[0], pcv, z) + SWP(pcv) * pa[1];
    }
  };
  auto pfx_store = [&](int ts) AI {
    const int cp = (tb0 + ts) * 32 + (olane() & 31);
    char* p = ws + OFF_CAR + (size_t)(pi * 1024 + cp) * 64;
#pragma unroll
    for (int g = 0; g < 4; ++g) {
      const int a = g & 2, o = 2 * (g & 1);
      *(f32x4*)(p + g * 16) = (f32x4){pz[a][o], pz[a + 1][o], pz[a][o + 1], pz[a + 1][o + 1]};
    }
    *(float*)(ws + OFF_TOT + (size_t)(cp * 16 + pi) * 4) = pcv;
  };
  auto quantum = [&](auto uc) AI {
    constexpr int u = decltype(uc)::value;
    if constexpr (u < 10) stat_q(u);
    if constexpr (GT) {
      if constexpr (u == 4) cs_get(4 * cg);
      if constexpr (u == 45) cs_get(4 * cg + 1);
      if constexpr (u == 77) cs_get(4 * cg + 2);
      if constexpr (u == 110) cs_get(4 * cg + 3);
      if constexpr (u == 6 || u == 46 || u == 78 || u == 111) cs_red();
      if constexpr (u == 8) cq_load(4 * cg, 0);
      if constexpr (u >= 10 && u < 42) gate_q(0, u - 10);
      if constexpr (u == 18 || u == 19) ss_send(u - 18);
      if constexpr (u == 47) cq_load(4 * cg + 1, 0);
      if constexpr (u >= 49 && u < 57) up_q(1, u - 49);
      if constexpr (u == 53) up_st(1, 0);
      if constexpr (u == 57) { up_st(1, 1); ssm_pre(8, 0.5f); ss_recv(); ssm_mr(0, 0); }
      if constexpr (u >= 59 && u < 75) ss_q(u - 59);
      if constexpr (u == 75) ssm_st(8, 0);
      if constexpr (u == 79) { cq_load(4 * cg + 2, 0); pfx_load(8); }
      if constexpr (u >= 81) gate_q(2, u - 81);
      if constexpr (u >= 82 && u < 101) pfx_step(u - 82);
      if constexpr (u == 102) { if (w == 0 && olane() < 32) pfx_store(8); }
    } else {
      sfor<0, 4>([&](auto pc_) AI {
        constexpr int p = decltype(pc_)::value;
        if constexpr (u == k1_s_unit(p, 0) - (p > 0 ? 1 : 0)) { ssm_pre(4 * cg + p, 1.f); ssm_mr(0, 0); ssm_mr(1, 0); }
        sfor<0, 32>([&](auto hc) AI {
          constexpr int hq = decltype(hc)::value;
          if constexpr (u == k1_s_unit(p, hq)) ssm_q(p, hq);
        });
      });
    }
  };

  unsigned as0, as2;
  bf16x8 f[4];
  auto rd = [&](auto uc) AI {
    constexpr int u = decltype(uc)::value;
    if constexpr (u < 112) {
      constexpr int s = 8 + (u >> 4), k = u & 15, so = (s & 1) * 32768;
      if constexpr (s == SSLOT) {
        if (k == 0) {
          as0 = sbase(olane()) + (s & 2) * 32768;
          as2 = as0 ^ 64;
        }
        f[u & 3] = *LP(bf16x8, ((k & 1) ? as2 : as0) + so + (k >> 1) * 4096);
      } else {
        f[u & 3] = wfrag(aw[k & 3] + so + (k >> 2) * 8192);
      }
      if ((s & 1) && k == 15 && s < 14) flip();
    }
  };
  RBAR(8, k1_wait(GT, 8));
  STAMP(4);
  rd(IC<0>{}); rd(IC<1>{}); rd(IC<2>{}); rd(IC<3>{});
  SCHED();
  p1tail(7);
  f32x4 csb = zero4;
  SCHED();
  sfor<0, 112>([&](auto uc) AI {
    constexpr int u = decltype(uc)::value;
    constexpr int s = 8 + (u >> 4), k = u & 15;
    constexpr bool ss = s == SSLOT;
    constexpr int sp = GT ? s - 9 : s - 8, p = 1 + (sp >> 1), half = sp & 1;
    constexpr bool first = k == 0 && (ss || half == 0);
    if constexpr (ss) {
      if (!(k & 1)) csb = MFMA16(sel, f[u & 3], first ? zero4 : csb);
      accs = MFMA32(xf[2 * k], f[u & 3], first ? zero16 : accs);
    } else {
      if (!(k & 3)) csb = MFMA16(sel, f[u & 3], first ? zero4 : csb);
      acc[p] = mm(f[u & 3], xf[16 * half + k], first ? zero16 : acc[p]);
    }
    SCHED();
    if constexpr (!ss && half == 1 && k == 13) { csa = csb; cs_put(4 * cg + p); }
    if constexpr (ss && k == 15) { csa = csb; cs_put(8); }
    if constexpr (k == 13 && s < 14) {
      if (s == 10) STAMP(11);
      if (s == 12) STAMP(12);
      RBAR(s + 1, k1_wait(GT, s + 1));
      STAMP(s - 3);
      rd(IC<u + 3>{}); rd(IC<u + 4>{});
    } else if constexpr (k != 12 || s == 14) {
      rd(IC<u + 4>{});
    }
    if constexpr (u == 4) BARRIER();
    if constexpr (GT && u == 110) { BARRIER(); }
    constexpr int t = k1_ival(u);
    if constexpr (t + 3 < 15) {
#pragma unroll
      for (int d = 0; d < 4; ++d)
        if (u == k1_dma_unit(t, d)) issue(IC<t + 3>{}, d);
    }
#pragma unroll
    for (int i = 0; i < K1_NPF; ++i)
      if (u == k1_pf_unit(i)) prefetch(i);
    quantum(uc);
    SCHED();
  });
  STAMP(13);
  if (GT) {
    gate_q(2, 31);
    cq_load(4 * cg + 3, 0);
#pragma unroll
    for (int k = 0; k < 8; ++k) {
      up_q(3, k);
      if (k == 3) up_st(3, 0);
    }
    up_st(3, 1);
  } else {
    ssm_q(3, 30); ssm_q(3, 31);
    ss_send(0); ss_send(1);
    BARRIER();
    ssm_pre(8, 0.5f); ss_recv(); ssm_mr(0, 0);
#pragma unroll
    for (int hq = 0; hq < 16; ++hq) ss_q(hq);
    ssm_st(8, 0);
    BARRIER();
    const int ts = 2 * w + (lane >> 5);
    if (ts < 9) {
      pfx_load(ts);
#pragma unroll
      for (int k = 0; k < 19; ++k) pfx_step(k);
      pfx_store(ts);
    }
  }
  STAMP(14);
  VMCNT(0);
  RT1(0, (pi * 16 + nb) * 8 + w, ws);
}

__global__ __launch_bounds__(512, 2) NOPK void k1(K1Args A) {
  extern __shared__ __attribute__((aligned(16))) char smem[];
  const int b = blockIdx.x;
  const int xcd = b & 7, r = b >> 3;
  const int pl = r & 7, nl = r >> 3;
  const int pi = (xcd & 1) * 8 + pl, nb = (xcd >> 1) * 4 + nl;
  const int c0 = (pl + 2 * nl) & 7;
  unsigned long long cpc;
  unsigned clen;
  asm volatile("s_getpc_b64 %0\nk1_pc_ref:\n\ts_mov_b32 %1, k2_code_end-k1_pc_ref" : "=s"(cpc), "=s"(clen));
  clen = clen < 65536u ? clen : 0u;
  if (nb < 14) k1_body<true>(A, smem, pi, nb, c0, r, cpc, clen);
  else k1_body<false>(A, smem, pi, nb, c0, r, cpc, clen);
}

struct K2Args {
  const u16 *x, *Dk, *wd, *Cre, *Cim;
  char* ws;
  float* out;
};
constexpr int L2_EXS = 131072;
constexpr int L2_EXY = 65536;
constexpr int L2_X = L2_EXS + 16384;
constexpr int L2_SIZE = L2_X + 10240;
#define AY 0
#define XT 6
#define YSC " sc1"
template <int S> __device__ __forceinline__ void dmac(const char* sbase, unsigned voff, unsigned lds_dst) {
  asm volatile("s_mov_b32 m0, %2\n\ts_nop 0\n\tglobal_load_lds_dwordx4 %0, %1" YSC " ; RING %3" ::"v"(voff), "s"(sbase), "s"(lds_dst), "i"(S) : "memory");
}
#define SBAR() do { asm volatile("" ::: "memory"); __builtin_amdgcn_s_barrier(); asm volatile("" ::: "memory"); } while (0)
#define K2ST(k, i)
#define K2OUT(k, w)
#define K2DECL
__device__ __forceinline__ void k2_load(const K2Args& A, unsigned lds0, int w, int ml, int mi, int nj) {
  const int lane = threadIdx.x & 63;
  K2DECL;
  K2ST(3, 0);
  char* ws = A.ws;
  const int r8 = lane >> 3, pc = lane & 7;
  const int rotg = (7 * ml) >> 1;
  const unsigned va = w * 2048 + lane * 16;
  const unsigned vw = (16 * w + r8) * 1024 + 16 * (pc ^ (((r8 >> 1) & 1) << 2));
  const unsigned vc = (8 * w + r8) * 1024 + 16 * (pc ^ ((4 * (w & 1) + (r8 >> 1)) & 7));
  auto issue = [&](auto sc) AI {
    constexpr int s = decltype(sc)::value;
    const unsigned sl = lds0 + (s & 3) * 32768;
    if constexpr (s >= 22) {
    } else if constexpr (s < 14) {
      const int gs = s + rotg >= 14 ? s + rotg - 14 : s + rotg;
      const char* ab = ws + OFF_A + (size_t)(mi * 14 + gs) * 16384;
      const char* wb = (const char*)A.wd + (size_t)gs * 131072 + nj * 128;
      dmar<s>(ab, va, sl + w * 2048);
      dmar<s>(ab + 1024, va, sl + w * 2048 + 1024);
      dmar<s>(wb, vw, sl + 16384 + w * 2048);
      dmar<s>(wb + 8192, vw, sl + 16384 + w * 2048 + 1024);
    } else {
      const int ys = (s - 14 + nj) & 7;
      const char* hb = ws + OFF_HF + (size_t)(mi * 8 + ys) * 16384;
      dmac<s>(hb, va, sl + w * 2048);
      dmac<s>(hb + 1024, va, sl + w * 2048 + 1024);
      dmar<s>((const char*)A.Cre + nj * 65536 + ys * 128, vc, sl + 16384 + w * 1024);
      dmar<s>((const char*)A.Cim + nj * 65536 + ys * 128, vc, sl + 24576 + w * 1024);
    }
  };
  {
    const int s0 = w >> 2;
    const int gs = s0 + rotg;
    const unsigned va0 = lane * 16, vw0 = r8 * 1024 + 16 * (pc ^ (((r8 >> 1) & 1) << 2));
    const char* ab = ws + OFF_A + (size_t)(mi * 14 + gs) * 16384 + (w & 3) * 4096;
    const char* wb = (const char*)A.wd + (size_t)gs * 131072 + nj * 128 + (w & 3) * 32768;
    const unsigned sl = lds0 + s0 * 32768 + (w & 3) * 4096;
#pragma unroll
    for (int d = 0; d < 4; ++d) {
      dma(ab + d * 1024, va0, sl + d * 1024);
      dma(wb + d * 8192, vw0, sl + 16384 + d * 1024);
    }
  }
  K2ST(3, 2);
  issue(IC<2>{});
  K2ST(3, 3);
  if (w < 4) VMCNT(4);
  SBAR();
  K2ST(3, 4);
  sfor<0, 21>([&](auto tc) AI {
    constexpr int t = decltype(tc)::value;
    if constexpr (t == XT) {
      dma((const char*)A.x + (size_t)mi * 65536 + nj * 128, (8 * w + r8) * 1024 + pc * 16, lds0 + L2_X + w * 1024);
      if (w == 0) dma(ws + OFF_ST + mi * 512, (lane & 31) * 16, lds0 + L2_X + 8192);
      if (w == 1) dma((const char*)A.Dk + nj * 128, pc * 16, lds0 + L2_X + 9216);
    }
    issue(IC<t + 3>{});
    RBAR(t + 1, (t + 2 < 22 ? 4 : 0) + (t + 3 < 22 ? 4 : 0));
    if (t & 1) K2ST(3, 5 + (t >> 1));
    if (t == 20) K2ST(3, 15);
  });
  K2OUT(3, w);
}

__device__ __forceinline__ float mulc(f32x2 c, float v) { return fmaf(c[0], v, SWP(v) * c[1]); }
__device__ __forceinline__ float fmac(f32x2 c, float v, float t) { return fmaf(SWP(v), c[1], fmaf(c[0], v, t)); }
__device__ __forceinline__ float bfadd(unsigned pair, unsigned selv, float c) {
  return __builtin_amdgcn_fdot2_f32_bf16(__builtin_bit_cast(bf16x2_t, pair), __builtin_bit_cast(bf16x2_t, selv), c, false);
}

struct K2Out {
  float sgv[8], xfv[8], rs;
  u32x2 xq[2], dq;
  f32x2 st[2];
  __device__ __forceinline__ void load(unsigned lds0, int w, int eh, int lane) {
    const int r = 32 * (w & 1) + 16 * eh + (lane >> 3);
    const unsigned co = (w >> 1) * 64 + (lane & 7) * 8;
#pragma unroll
    for (int e = 0; e < 2; ++e) {
      xq[e] = *LP(u32x2, lds0 + L2_X + (r + 8 * e) * 128 + co);
      st[e] = *LP(f32x2, lds0 + L2_X + 8192 + (r + 8 * e) * 8);
    }
    dq = *LP(u32x2, lds0 + L2_X + 9216 + co);
  }
  __device__ __forceinline__ void prep(int v) {
    const int e = v >> 2;
    if (!(v & 3)) rs = __builtin_amdgcn_rcpf(st[e][1]);
    const unsigned xw = xq[e][(v >> 1) & 1], dw = dq[(v >> 1) & 1];
    const float xx = bf2f((xw >> (16 * (v & 1))) & 0xffff), dd = bf2f((dw >> (16 * (v & 1))) & 0xffff);
    const float xn = rbf(rbf(xx - st[e][0]) * rs);
    xfv[v] = xx;
    sgv[v] = rbf(xn * dd);
    asm volatile("" : "+v"(xfv[v]), "+v"(sgv[v]));
  }
  __device__ __forceinline__ void finish(float* out, unsigned lds0, int w, int eh, int lane, int mi, int nj) {
    const int r8 = lane >> 3, pc = lane & 7;
    const unsigned exr = lds0 + w * 4096 + eh * 2048 + r8 * 128 + ((pc ^ r8) * 16);
    const unsigned eo = (64 * mi + 32 * (w & 1) + 16 * eh + r8) * D_ + 64 * nj + 32 * (w >> 1) + 4 * pc;
#pragma unroll
    for (int e = 0; e < 2; ++e) {
      const f32x4 y = *LP(f32x4, exr + L2_EXY + e * 1024), sv = *LP(f32x4, exr + L2_EXS + e * 1024);
      f32x4 o;
#pragma unroll
      for (int rr = 0; rr < 4; ++rr) o[rr] = fmaf(fmaf(2.f, y[rr], sgv[4 * e + rr]), sv[rr], xfv[4 * e + rr]);
      *(f32x4*)(out + eo + 8 * e * D_) = o;
    }
  }
};

__device__ __forceinline__ void k2_gate(const K2Args& A, unsigned lds0, int w, int mi, int nj) {
  const int lane = threadIdx.x & 63;
  char* ws = A.ws;
  K2DECL;
  K2ST(2, 0);
  RT0();
  const int wr = w & 1, wc = w >> 1;
  const int n = lane & 31, h = lane >> 5, t16 = lane & 15, g1 = (lane >> 4) & 1;
  const unsigned exw = lds0 + w * 4096 + n * 128 + ((h ^ (n & 7)) * 16);
  const unsigned arb = lds0 + wr * 1024 + lane * 16;
  const unsigned trw = lds0 + 16384 + (4 * h + (t16 >> 2)) * 128 + 32 * ((2 * wc + g1) ^ ((t16 >> 3) << 1)) + 8 * (t16 & 3);
  f32x16 acc;
#pragma unroll
  for (int i = 0; i < 16; ++i) acc[i] = 0.f;
  bf16x8 fa[4], fb[4];
  K2Out O;
  unsigned ga[2] = {arb, arb + 65536}, gb[2] = {trw, trw + 65536};
  asm volatile("" : "+v"(ga[1]), "+v"(gb[1]));
  auto rd = [&](auto uc) AI {
    constexpr int u = decltype(uc)::value, s = u >> 3, j = u & 7, so = (s & 1) * 32768, hf = (s >> 1) & 1;
    if constexpr (u < 112) {
      fa[u & 3] = *LP(bf16x8, ga[hf] + so + j * 2048);
      fb[u & 3] = trfrag(gb[hf] + so + j * 2048, 1024);
    }
  };
  SBAR();
  K2ST(2, 1);
  rd(IC<0>{}); rd(IC<1>{}); rd(IC<2>{});
  SCHED();
  sfor<0, 112>([&](auto uc) AI {
    constexpr int u = decltype(uc)::value;
    acc = MFMA32(fb[u & 3], fa[u & 3], acc);
    SCHED();
    if constexpr ((u & 7) == 5) BARRIER();
    rd(IC<u + 3>{});
    if constexpr (u == 74) O.load(lds0, w, 0, lane);
    if constexpr (u >= 78 && u < 110 && (u & 3) == 2) O.prep((u - 78) >> 2);
    SCHED();
    if ((u & 15) == 15) K2ST(2, 2 + (u >> 4));
  });
#pragma unroll
  for (int q = 0; q < 6; ++q) {
#pragma unroll
    for (int v = (16 * q) / 6; v < (16 * q + 16) / 6; ++v) {
      const float g = rbf(acc[v]);
      acc[v] = rbf(g * __builtin_amdgcn_rcpf(1.f + __expf(-g)));
    }
    asm volatile("" : "+v"(acc));
    SBAR();
  }
  SBAR();
#pragma unroll
  for (int g = 0; g < 4; ++g) {
    const f32x4 o = {acc[4 * g], acc[4 * g + 1], acc[4 * g + 2], acc[4 * g + 3]};
    const unsigned a = exw ^ (g << 5);
    asm volatile("ds_write_b128 %0, %1" ::"v"(a + L2_EXS), "v"(o) : "memory");
  }
  K2ST(2, 9);
  BARRIER();
  K2ST(2, 10);
  O.finish(A.out, lds0, w, 0, lane, mi, nj);
  K2ST(2, 11);
  RT1(1, blockIdx.x * 8 + w, ws);
  RT1(1, blockIdx.x * 8 + w + 4, ws);
}

__device__ __forceinline__ void k2_state(const K2Args& A, unsigned lds0, int w, int mi, int nj) {
  const int lane = threadIdx.x & 63, pi = mi >> 1;
  char* ws = A.ws;
  const int sg = lane >> 4;
  const unsigned cp = nj * 128 + 32 * w + (lane & 15);
  const unsigned hoff = w * 4096 + sg * 512 + (((lane & 15) ^ (sg & 1) ^ ((sg >> 1) << 3)) * 16);
  f32x2 a1[2], cr[2];
  f32x4 tq[2];
  u32x4 hq[2][2];
  {
    const unsigned o0 = cp * 8, o1 = cp * 64 + (12 - 4 * sg) * 4, o2 = cp * 64 + sg * 8;
    const char* b0 = ws + OFF_AT;
    const char* b0b = b0 + 128;
    GLD8S(a1[0], o0, b0); GLD8S(a1[1], o0, b0b);
#pragma unroll
    for (int j = 0; j < 2; ++j) {
      const char* b1 = ws + OFF_TOT + (pi - 16) * 4 + j * 1024;
      const char* b2 = ws + OFF_CAR + pi * 65536 + (mi & 1) * 32 + j * 1024;
      GLD16S(tq[j], o1, b1); GLD8S(cr[j], o2, b2);
    }
  }
  auto hload = [&](int j, int c) AI {
    const char* b3 = ws + OFF_H + (size_t)(mi * 8 + nj) * 16384 + j * 256 + c * 2048;
    GLD16S(hq[j][c], hoff, b3);
  };
  asm volatile("s_waitcnt vmcnt(4)" : "+v"(a1[0]), "+v"(a1[1])::"memory");
  f32x2 a8[2], a16[2], a32[2], a64[2], aP[2], fF[2], fG[2];
#pragma unroll
  for (int j = 0; j < 2; ++j) {
    a8[j] = csq(csq(csq(a1[j]))); a16[j] = csq(a8[j]); a32[j] = csq(a16[j]); a64[j] = csq(a32[j]); aP[j] = csq(a64[j]);
  }
  asm volatile("" : "+v"(aP[0]), "+v"(aP[1]));
  SBAR();
  hload(0, 0); hload(0, 1);
  const f32x2 one = {1.f, 0.f};
#pragma unroll
  for (int j = 0; j < 2; ++j) {
    const f32x2 p4 = csq(csq(aP[j])), p8 = csq(p4);
    fF[j] = cmul(LB4 ? p4 : one, LLO ? one : p8);
    fG[j] = cmul(cmul(a1[j], (mi & 1) ? a64[j] : one), cmul(LB4 ? a8[j] : one, LLO ? one : a16[j]));
  }
  asm volatile("" : "+v"(fF[0]), "+v"(fF[1]), "+v"(fG[0]), "+v"(fG[1]));
  SBAR();
  hload(1, 0); hload(1, 1);
  asm volatile("s_waitcnt vmcnt(4)" : "+v"(cr[0]), "+v"(cr[1]), "+v"(tq[0]), "+v"(tq[1])::"memory");
  unsigned slo = 0x00003F80u, shi = 0x3F800000u;
  asm volatile("" : "+s"(slo), "+s"(shi));
  float pr[2][2];
  {
    const int i0 = pi - 4 - 4 * sg;
    float hs[2], b[2];
#pragma unroll
    for (int j = 0; j < 2; ++j) {
      hs[j] = i0 >= 0 ? tq[j][0] : 0.f;
#pragma unroll
      for (int m = 1; m < 4; ++m) hs[j] = fmac(aP[j], hs[j], i0 + m >= 0 ? tq[j][m] : 0.f);
      hs[j] = mulc(fF[j], hs[j]);
      b[j] = hs[j];
    }
    asm volatile("s_nop 1\n\tv_permlane16_swap_b32 %0, %1\n\tv_permlane16_swap_b32 %2, %3" : "+v"(hs[0]), "+v"(b[0]), "+v"(hs[1]), "+v"(b[1]));
    hs[0] += b[0]; hs[1] += b[1];
    b[0] = hs[0]; b[1] = hs[1];
    asm volatile("s_nop 1\n\tv_permlane32_swap_b32 %0, %1\n\tv_permlane32_swap_b32 %2, %3" : "+v"(hs[0]), "+v"(b[0]), "+v"(hs[1]), "+v"(b[1]));
#pragma unroll
    for (int j = 0; j < 2; ++j) {
      const float g0 = mulc(fG[j], hs[j] + b[j]), g1v = mulc(a32[j], g0);
      pr[j][0] = fmac(a1[j], cr[j][0], g0);
      pr[j][1] = fmac(a1[j], cr[j][1], g1v);
    }
    asm volatile("" : "+v"(pr[0][0]), "+v"(pr[0][1]), "+v"(pr[1][0]), "+v"(pr[1][1]));
  }
  SBAR();
#pragma unroll
  for (int j = 0; j < 2; ++j) {
    asm volatile("s_waitcnt vmcnt(2)" : "+v"(hq[j][0]), "+v"(hq[j][1])::"memory");
#pragma unroll
    for (int q = 0; q < 4; ++q) {
#pragma unroll
      for (int c = 0; c < 2; ++c) {
        const float p1 = mulc(a1[j], pr[j][c]);
        hq[j][c][q] = pk2(bfadd(hq[j][c][q], slo, pr[j][c]), bfadd(hq[j][c][q], shi, p1));
        if (q < 3) pr[j][c] = mulc(a1[j], p1);
      }
    }
#pragma unroll
    for (int c = 0; c < 2; ++c) {
      const char* sb = ws + OFF_HF + (size_t)(mi * 8 + nj) * 16384 + j * 256 + c * 2048;
      GST16S(sb, hoff, hq[j][c]);
    }
    SBAR();
  }
  SBAR();
  SBAR();
  VMCNT(0);
  SBAR();
  const char* cb = ws + OFF_CNT + mi * 128;
  const unsigned vz = 0;
  unsigned cnt;
  if (w == 0 && lane == 0) asm volatile("global_atomic_add %0, %1, %2 sc1" ::"v"(vz), "v"(1u), "s"(cb) : "memory");
  SBAR();
  SBAR();
  if (w == 0) asm volatile("global_load_dword %0, %1, %2 sc1" : "=v"(cnt) : "v"(vz), "s"(cb) : "memory");
  const int wr = w & 1, wc = w >> 1;
  const int n = lane & 31, h = lane >> 5, t16 = lane & 15, g1 = (lane >> 4) & 1;
  const unsigned exw = lds0 + w * 4096 + n * 128 + ((h ^ (n & 7)) * 16);
  const unsigned trh = lds0 + (4 * wr + 2 * g1 + ((t16 >> 1) & 1)) * 512 + ((16 * h + 8 * g1 + 2 * (t16 >> 2) + ((t16 >> 1) & 1)) * 16) + (t16 & 1) * 8;
  const int cd = 32 * wc + n;
  const unsigned crb = lds0 + 16384 + cd * 128 + 16 * (h ^ ((cd >> 1) & 7));
  unsigned ya[2][4], yc[2][4];
#pragma unroll
  for (int i = 0; i < 4; ++i) {
    ya[0][i] = trh ^ ((i & 1) * 16) ^ ((i >> 1) * 128); ya[1][i] = ya[0][i] + 65536;
    yc[0][i] = crb ^ (i << 5); yc[1][i] = yc[0][i] + 65536;
  }
  asm volatile("" : "+v"(ya[1][0]), "+v"(ya[1][1]), "+v"(ya[1][2]), "+v"(ya[1][3]));
  asm volatile("" : "+v"(yc[1][0]), "+v"(yc[1][1]), "+v"(yc[1][2]), "+v"(yc[1][3]));
  SBAR();
  if (w == 0) {
    asm volatile("s_waitcnt vmcnt(0)" : "+v"(cnt)::"memory");
    while (__builtin_amdgcn_readfirstlane(cnt) < 8u) {
      __builtin_amdgcn_s_sleep(2);
      asm volatile("global_load_dword %0, %1, %2 sc1\n\ts_waitcnt vmcnt(0)" : "=v"(cnt) : "v"(vz), "s"(cb) : "memory");
    }
  }
  SBAR();
  K2Out O;
  O.load(lds0, w, 1, lane);
#pragma unroll
  for (int v = 0; v < 3; ++v) O.prep(v);
  SBAR();
#pragma unroll
  for (int v = 3; v < 6; ++v) O.prep(v);
  SBAR();
#pragma unroll
  for (int v = 6; v < 8; ++v) O.prep(v);
  f32x16 acc;
#pragma unroll
  for (int i = 0; i < 16; ++i) acc[i] = 0.f;
  bf16x8 fa[4], fb[4];
  auto rd = [&](auto uc) AI {
    constexpr int u = decltype(uc)::value, s = 14 + (u >> 3), i = (u >> 1) & 3, part = u & 1, so = (s & 1) * 32768, hf = (s >> 1) & 1;
    if constexpr (u < 64) {
      fa[u & 3] = trfrag2(ya[hf][part] + so + i * 4096, ya[hf][2 + part] + so + i * 4096);
      fb[u & 3] = *LP(bf16x8, yc[hf][i] + so + part * 8192);
    }
  };
  __builtin_amdgcn_s_setprio(2);
  SBAR();
  rd(IC<0>{}); rd(IC<1>{}); rd(IC<2>{});
  SCHED();
  sfor<0, 64>([&](auto uc) AI {
    constexpr int u = decltype(uc)::value;
    acc = MFMA32(fb[u & 3], fa[u & 3], acc);
    SCHED();
    if constexpr ((u & 7) == 5 && u < 56) BARRIER();
    rd(IC<u + 3>{});
    SCHED();
  });
#pragma unroll
  for (int g = 0; g < 4; ++g)
    *LP(f32x4, (exw ^ (g << 5)) + L2_EXY) = (f32x4){acc[4 * g], acc[4 * g + 1], acc[4 * g + 2], acc[4 * g + 3]};
  BARRIER();
  O.finish(A.out, lds0, w, 1, lane, mi, nj);
}

__global__ __launch_bounds__(1024, 4) NOPK void k2(K2Args A) {
  extern __shared__ __attribute__((aligned(16))) char smem[];
  const unsigned lds0 = (unsigned)(uintptr_t)(LAS char*)smem;
  const int w = __builtin_amdgcn_readfirstlane(threadIdx.x >> 6) & 15;
  const int b = blockIdx.x;
  const int xcd = b & 7, r = b >> 3;
  const int ml = r >> 3, mi = xcd * 4 + ml, nj = r & 7;
  if (w < 4) {
    k2_state(A, lds0, w, mi, nj);
  } else if (w < 12) {
    k2_load(A, lds0, w - 4, ml, mi, nj);
  } else {
    __builtin_amdgcn_s_setprio(1);
    k2_gate(A, lds0, w & 3, mi, nj);
  }
  asm volatile("k2_code_end:");
}

extern "C" void kernel_launch(void* const* d_in, const int* in_sizes, int n_in, void* d_out, int out_size, void* d_ws,
                              size_t ws_size, hipStream_t stream) {
  K1Args a;
  a.x = (const u16*)d_in[0]; a.Lre = (const u16*)d_in[1]; a.Lim = (const u16*)d_in[2];
  a.Bre = (const u16*)d_in[3]; a.Bim = (const u16*)d_in[4]; a.Cre = (const u16*)d_in[5]; a.Cim = (const u16*)d_in[6];
  a.ldt = (const u16*)d_in[7]; a.wg = (const u16*)d_in[10]; a.wu = (const u16*)d_in[11]; a.wd = (const u16*)d_in[12];
  a.ws = (char*)d_ws;
  (void)hipFuncSetAttribute((const void*)k1, hipFuncAttributeMaxDynamicSharedMemorySize, L1_SIZE);
  (void)hipFuncSetAttribute((const void*)k2, hipFuncAttributeMaxDynamicSharedMemorySize, L2_SIZE);
  k1<<<256, 512, L1_SIZE, stream>>>(a);
  K2Args b;
  b.x = (const u16*)d_in[0]; b.Dk = (const u16*)d_in[8]; b.wd = (const u16*)d_in[12];
  b.Cre = (const u16*)d_in[5]; b.Cim = (const u16*)d_in[6];
  b.ws = (char*)d_ws; b.out = (float*)d_out;
  k2<<<256, 1024, L2_SIZE, stream>>>(b);
}
```

```cpp
#include <hip/hip_runtime.h>
#include <hip/hip_bf16.h>
#include <stdint.h>

typedef unsigned short u16;
typedef __attribute__((ext_vector_type(8))) short bf16x8;
typedef __attribute__((ext_vector_type(4))) short s16x4;
typedef __attribute__((ext_vector_type(16))) float f32x16;
typedef __attribute__((ext_vector_type(4))) float f32x4;
typedef __attribute__((ext_vector_type(2))) float f32x2;
typedef __attribute__((ext_vector_type(4))) unsigned u32x4;
typedef __attribute__((ext_vector_type(2))) unsigned u32x2;
typedef __attribute__((ext_vector_type(2))) __bf16 bf16x2_t;
#define LAS __attribute__((address_space(3)))
#if defined(__HIP_DEVICE_COMPILE__)
#define NOPK __attribute__((target("no-packed-fp32-ops")))
#else
#define NOPK
#endif

constexpr int D_ = 512;

constexpr size_t OFF_A   = 0;
constexpr size_t OFF_H   = OFF_A + (size_t)32 * 112 * 2048;
constexpr size_t OFF_CAR = OFF_H + (size_t)32 * 32 * 4096;
constexpr size_t OFF_TOT = OFF_CAR + (size_t)16 * 1024 * 64;
constexpr size_t OFF_AT  = OFF_TOT + (size_t)16 * 4096;
constexpr size_t OFF_ST  = OFF_AT + 16384;
constexpr size_t OFF_CNT = OFF_ST + 16384;
constexpr size_t OFF_T0  = OFF_CNT + 4096;
constexpr size_t OFF_HF  = OFF_T0 + 65536;

constexpr int L1_AGG = 131072;
constexpr int L1_AC  = L1_AGG + 18432;
constexpr int L1_STF = L1_AC + 4608;
constexpr int L1_ST  = L1_STF + 1024;
constexpr int L1_CS  = L1_ST + 2048;
constexpr int L1_DUM = L1_CS + 5120;
constexpr int L1_SIZE = L1_DUM + 1024;

__device__ __forceinline__ float bf2f(unsigned h) { return __builtin_bit_cast(float, h << 16); }
__device__ __forceinline__ float rbf(float v) { return (float)(__bf16)v; }
__device__ __forceinline__ unsigned pk2(float lo, float hi) {
  bf16x2_t r = {(__bf16)lo, (__bf16)hi};
  return __builtin_bit_cast(unsigned, r);
}
template <int CTRL> __device__ __forceinline__ float dpp0(float x) {
  return __builtin_bit_cast(float, __builtin_amdgcn_update_dpp(0, __builtin_bit_cast(int, x), CTRL, 0xf, 0xf, true));
}
#define SWP(x) dpp0<0xB1>(x)
__device__ __forceinline__ f32x2 csq(f32x2 a) { return (f32x2){fmaf(a[0], a[0], -a[1] * a[1]), 2.f * a[0] * a[1]}; }
__device__ __forceinline__ f32x2 cmul(f32x2 a, f32x2 b) {
  return (f32x2){fmaf(a[0], b[0], -a[1] * b[1]), fmaf(a[0], b[1], a[1] * b[0])};
}
__device__ __forceinline__ f32x2 cfma(f32x2 a, f32x2 b, f32x2 c) {
  return (f32x2){fmaf(a[0], b[0], fmaf(-a[1], b[1], c[0])), fmaf(a[0], b[1], fmaf(a[1], b[0], c[1]))};
}
template <int V> struct IC { static constexpr int value = V; };
template <int I, int N, class F> __device__ __forceinline__ void sfor(F&& f) {
  if constexpr (I < N) { f(IC<I>{}); sfor<I + 1, N>(f); }
}
__device__ __forceinline__ const char* uptr(const void* base, unsigned off) {
  return (const char*)base + (size_t)(unsigned)__builtin_amdgcn_readfirstlane(off);
}
#define MFMA16(a, b, c) __builtin_amdgcn_mfma_f32_16x16x32_bf16(a, b, c, 0, 0, 0)
#define MFMA32(a, b, c) __builtin_amdgcn_mfma_f32_32x32x16_bf16(a, b, c, 0, 0, 0)
#define SCHED() __builtin_amdgcn_sched_barrier(0)
#define LP(T, a) ((LAS T*)(unsigned)(a))
#define AI __attribute__((always_inline))
#define LMASK(m) __builtin_amdgcn_inverse_ballot_w64(m)
#define LB0 LMASK(0xAAAAAAAAAAAAAAAAull)
#define LB1 LMASK(0xCCCCCCCCCCCCCCCCull)
#define LB3 LMASK(0xFF00FF00FF00FF00ull)
#define LB4 LMASK(0xFFFF0000FFFF0000ull)
#define LLO LMASK(0x00000000FFFFFFFFull)

#define GLD16(dst, p) asm volatile("global_load_dwordx4 %0, %1, off" : "=v"(dst) : "v"(p) : "memory")
#define GLD8(dst, p) asm volatile("global_load_dwordx2 %0, %1, off" : "=v"(dst) : "v"(p) : "memory")
#define GLD2(dst, p) asm volatile("global_load_ushort %0, %1, off" : "=v"(dst) : "v"(p) : "memory")
#define GLD16S(dst, vo, sb) asm volatile("global_load_dwordx4 %0, %1, %2" : "=v"(dst) : "v"(vo), "s"(sb) : "memory")
#define GLD8S(dst, vo, sb) asm volatile("global_load_dwordx2 %0, %1, %2" : "=v"(dst) : "v"(vo), "s"(sb) : "memory")
#define GST16S(sb, vo, v) asm volatile("global_store_dwordx4 %0, %1, %2 sc0 sc1\n\ts_nop 1" ::"v"(vo), "v"(v), "s"(sb) : "memory")
#define LDSW(a, off, v) asm volatile("ds_write_b128 %0, %1 offset:%2" ::"v"(a), "v"(v), "i"(off) : "memory")
#define VMCNT(n) asm volatile("s_waitcnt vmcnt(%0)" ::"i"(n) : "memory")
#define BARRIER() do { asm volatile("s_waitcnt lgkmcnt(0)" ::: "memory"); __builtin_amdgcn_s_barrier(); asm volatile("" ::: "memory"); } while (0)
#define RBAR(s, n) do { asm volatile("s_waitcnt vmcnt(%0) lgkmcnt(0) ; NEED %1" ::"i"(n), "i"(s) : "memory"); __builtin_amdgcn_s_barrier(); asm volatile("" ::: "memory"); } while (0)
__device__ __forceinline__ void dma(const char* sbase, unsigned voff, unsigned lds_dst) {
  asm volatile("s_mov_b32 m0, %2\n\ts_nop 0\n\tglobal_load_lds_dwordx4 %0, %1" ::"v"(voff), "s"(sbase), "s"(lds_dst) : "memory");
}
template <int S> __device__ __forceinline__ void dmar(const char* sbase, unsigned voff, unsigned lds_dst) {
  asm volatile("s_mov_b32 m0, %2\n\ts_nop 0\n\tglobal_load_lds_dwordx4 %0, %1 ; RING %3" ::"v"(voff), "s"(sbase), "s"(lds_dst), "i"(S) : "memory");
}
__device__ __forceinline__ bf16x8 trfrag2(unsigned a, unsigned b) {
  s16x4 lo = __builtin_amdgcn_ds_read_tr16_b64_v4i16(LP(s16x4, a));
  s16x4 hi = __builtin_amdgcn_ds_read_tr16_b64_v4i16(LP(s16x4, b));
  return __builtin_shufflevector(lo, hi, 0, 1, 2, 3, 4, 5, 6, 7);
}
__device__ __forceinline__ bf16x8 trfrag(unsigned a, int d) {
  s16x4 lo = __builtin_amdgcn_ds_read_tr16_b64_v4i16(LP(s16x4, a));
  s16x4 hi = __builtin_amdgcn_ds_read_tr16_b64_v4i16(LP(s16x4, a + d));
  return __builtin_shufflevector(lo, hi, 0, 1, 2, 3, 4, 5, 6, 7);
}

#define STAMP(i)
constexpr size_t OFF_DBG = (size_t)64 << 20;
#define RT0()
#define RT1(a,b,c)

struct K1Args {
  const u16 *x, *Lre, *Lim, *Bre, *Bim, *Cre, *Cim, *ldt, *wg, *wu, *wd;
  char* ws;
};

constexpr int k1_bar_unit(int s) { return 16 * (s - 8) - 3; }
constexpr int k1_ival(int u) { return 8 + (u + 3) / 16; }
constexpr int k1_nring(int s) { return s > 14 ? 0 : (s < 8 ? 3 : 4); }
constexpr int k1_dma_unit(int t, int d) { return t == 8 ? (d == 0 ? 1 : 4 * d - 2) : k1_bar_unit(t) + 1 + 4 * d; }
#define PFS 128
#define PT0 20
#define PF0 60
#define PFD 3
constexpr int K1_NPF = (90112 / (64 * PFS) + 7) / 8;
constexpr int K1_NPI = 13;
#define PNA 5
#define PA0 30
constexpr int k1_pf_unit(int i) { return i < PNA ? PA0 + PFD * i : PF0 + PFD * (i - PNA); }
constexpr int k1_s_unit(int p, int i) { return p == 0 ? 20 + (3 * i) / 2 : (p == 4 ? 45 + i : 22 + 30 * p + (3 * i) / 2); }
constexpr int k1_extra(bool gt, int u) {
  int n = 0;
  for (int i = 0; i < K1_NPI; ++i) n += gt && k1_pf_unit(i) == u;
  n += gt && u >= PT0 && u < PT0 + 3;
  if (gt) n += (u == 53) + (u == 57) + (u == 100);
  else n += (k1_s_unit(0, 14) == u) + (k1_s_unit(0, 15) == u) + (k1_s_unit(4, 7) == u) + (k1_s_unit(1, 14) + 2 == u) + (k1_s_unit(1, 15) + 2 == u) +
            (k1_s_unit(2, 14) == u) + (k1_s_unit(2, 15) == u);
  return n;
}
constexpr int k1_wait(bool gt, int s) {
  int n = k1_nring(s + 1) + k1_nring(s + 2);
  for (int u = s >= 11 ? k1_dma_unit(s - 3, 3) : 0; u < k1_bar_unit(s); ++u) n += k1_extra(gt, u);
  return n;
}

#define PFW 7
template <bool GT>
__device__ __forceinline__ void k1_body(const K1Args& A, char* smem, int pi, int nb, int c0, int rx, unsigned long long cpc,
                                        unsigned clen) {
  const int tid = threadIdx.x, lane = tid & 63;
  const int w = __builtin_amdgcn_readfirstlane(tid >> 6) & 7;
  const int rg = w & 3, cg = w >> 2, X = rg ^ cg;
  STAMP(0);
  RT0();
  const unsigned lds0 = (unsigned)(uintptr_t)(LAS char*)smem;
  const int r8 = lane >> 3, pc = lane & 7;
  const int tb0 = GT ? nb - 8 : 14 + 9 * (nb - 14);
  constexpr int SSLOT = 8;
  auto olane = [&]() AI -> int { int l = lane; asm volatile("" : "+v"(l)); return l; };

  const char* xb = uptr(A.x, pi * 131072 + w * 16384);
  const unsigned vs = r8 * 1024 + 16 * (pc ^ ((r8 >> 1) | ((w & 1) << 2)));
  const unsigned vw = GT ? (8 * w + r8) * 3584 + 16 * (pc ^ (((r8 >> 1) & 1) << 2)) : vs;
  const char* bmat = uptr((w & 1) ? A.Bim : A.Bre, ((GT ? nb : tb0) * 16 + 8 * ((w >> 1) & 1)) * 1024);
  auto wbase = [&](int p, int c) AI -> const char* {
    if (GT) return (const char*)((p & 1) ? A.wu : A.wg) + (size_t)c * (64 * 3584) + (128 * nb + 64 * (p >> 1)) * 2;
    return bmat + (unsigned)((4 * cg + p) * 16384 + c * 128);
  };
  auto issue = [&](auto sc, int d) AI {
    constexpr int s = decltype(sc)::value;
    const unsigned dst = lds0 + (s & 3) * 32768;
    if constexpr (s < 8) {
      const int c = (c0 + s) & 7;
      if (d == 0) dmar<s>(xb + c * 128, vs, dst + w * 2048);
      if (d == 1) dmar<s>(wbase(0, c), vw, dst + 16384 + w * 1024);
      if (d == 2) dmar<s>(xb + c * 128 + 8192, vs, dst + w * 2048 + 1024);
    } else if constexpr (s == SSLOT) {
      const int i = 4 * cg + d;
      dmar<s>(uptr(bmat, (GT ? 0 : 8 * 16384) + ((c0 + i) & 7) * 128), vs, dst + i * 4096 + rg * 1024);
    } else if constexpr (s < 15) {
      constexpr int sp = s - 9;
      dmar<s>(wbase(1 + (sp >> 1), (c0 + 4 * (sp & 1) + d) & 7), vw, dst + d * 8192 + w * 1024);
    }
  };
  const int gx = rx < 24 ? rx : 24 + (rx & 3);
  auto prefetch = [&](int i) AI {
    const char* sb = i < 8 ? (const char*)A.wd + (gx + 28 * i) * 8192
                           : (i < 12 ? (const char*)(i < 10 ? A.Cre : A.Cim) + (gx + 28 * (i & 1)) * 8192
                                     : (const char*)(gx < 8 ? A.Cre : A.Cim) + (56 + (gx & 7)) * 8192);
    const unsigned vo = olane() * 128;
    asm volatile("s_mov_b32 m0, %2\n\ts_cmp_eq_u32 %3, %4\n\ts_cselect_b64 exec, -1, 0\n\tglobal_load_lds_dword %0, %1\n\ts_mov_b64 exec, -1"
                 ::"v"(vo), "s"(sb), "s"(lds0 + L1_DUM), "s"(i == 12 ? w + (gx & 16) : w), "i"(i & 7) : "memory", "scc");
  };
  auto ptouch = [&](int i) AI {
    const char* sb = (const char*)(i == 0 ? A.wd : (i == 1 ? A.Cre : A.Cim)) + gx * 8192;
    const unsigned vo = olane() * (28 * 8192);
    asm volatile("s_mov_b32 m0, %2\n\ts_cmp_eq_u32 %3, %4\n\ts_cselect_b64 exec, %5, 0\n\tglobal_load_lds_dword %0, %1\n\ts_mov_b64 exec, -1"
                 ::"v"(vo), "s"(sb), "s"(lds0 + L1_DUM), "s"(w), "i"(5 + i), "i"(i == 0 ? 0xff : 3) : "memory", "scc");
  };
  {
    unsigned off = ((rx & 1) * 8 + w) * 4096 + lane * 64;
    off = off < clen ? off : 0;
    asm volatile("s_mov_b32 m0, %2\n\ts_nop 0\n\tglobal_load_lds_dword %0, %1" ::"v"(off), "s"(cpc), "s"(lds0 + L1_DUM) : "memory");
  }
  unsigned plr, pli, pdt;
  const int tsp = GT ? 8 : ((tid >> 5) < 8 ? (tid >> 5) : 8);
  {
    const int ch = 16 * (tb0 + tsp) + ((tid & 31) >> 1);
    GLD2(plr, A.Lre + ch); GLD2(pli, A.Lim + ch); GLD2(pdt, A.ldt + ch);
  }
  issue(IC<0>{}, 0); issue(IC<0>{}, 1); issue(IC<0>{}, 2);
  __builtin_amdgcn_s_barrier();
  issue(IC<1>{}, 0); issue(IC<1>{}, 1); issue(IC<1>{}, 2);
  __builtin_amdgcn_s_barrier();
  issue(IC<2>{}, 0); issue(IC<2>{}, 1); issue(IC<2>{}, 2);
  STAMP(1);
  char* ws = A.ws;
  float pe, pr_, pxx;
  f32x2 aco;
  int pq;
  auto param = [&](int k) AI {
    const float lr = bf2f(plr), li = bf2f(pli);
    if (k == 0) {
      asm volatile("" : "+v"(plr), "+v"(pli), "+v"(pdt));
      pxx = rbf(expf(bf2f(pdt)));
      asm volatile("" : "+v"(pxx));
    } else if (k == 1) {
      pe = expf(pxx * lr);
      const float th = pxx * li;
      const float kk = __builtin_rintf(th * 0.15915494f);
      const float r = fmaf(th, 6.4206383e-09f, fmaf(th, 0.15915494f, -kk));
      const float q = __builtin_rintf(4.f * r);
      pq = (int)q;
      pr_ = fmaf(-0.25f, q, r) * 6.2831855f;
      asm volatile("" : "+v"(pe), "+v"(pr_), "+v"(pq));
    } else if (k == 2) {
      const float z = pr_ * pr_;
      const float sn = fmaf(pr_ * z, fmaf(z, fmaf(z, -1.9515296e-4f, 8.3321609e-3f), -1.6666655e-1f), pr_);
      const float cs = fmaf(z * z, fmaf(z, fmaf(z, 2.4433157e-5f, -1.3887316e-3f), 4.1666646e-2f), fmaf(z, -0.5f, 1.f));
      const float c1 = (pq & 1) ? sn : cs, s1 = (pq & 1) ? cs : sn;
      aco = (f32x2){pe * (((pq + 1) & 2) ? -c1 : c1), pe * ((pq & 2) ? -s1 : s1)};
      asm volatile("" : "+v"(aco));
    } else if (k == 3) {
      const bool im = tid & 1;
      if (pi == 0) {
        const float ar = aco[0], ai = aco[1], nr = ar - 1.f, den = lr * lr + li * li;
        const float fr = (nr * lr + ai * li) / den, fi = (ai * lr - nr * li) / den;
        const f32x4 ac = {ar, im ? ai : -ai, im ? -fr : fr, -fi};
        if (tid < (GT ? 32 : 288)) *(f32x4*)(ws + OFF_AT + (size_t)((tb0 + tsp) * 32 + (tid & 31)) * 16) = ac;
      }
      aco[1] = im ? aco[1] : -aco[1];
      asm volatile("" : "+v"(aco));
      if (pi == 1 && nb == 0 && tid < 32) *(unsigned*)(ws + OFF_CNT + tid * 128) = 0u;
    }
  };

  const int n = lane & 31, h = lane >> 5, t16 = lane & 15;
  const int rho = 16 * ((n >> 2) & 1) + 4 * (n >> 3) + (n & 3);
  const int tau = rho ^ (8 * cg);
  const unsigned selw = (t16 == 4 * ((lane >> 4) & 1)) ? 0x3F803F80u : 0u;
  const bf16x8 sel = __builtin_bit_cast(bf16x8, (u32x4){selw, selw, selw, selw});
  const unsigned axb = lds0 + (32 * rg + tau) * 128 + 16 * ((2 * X + h) ^ (((tau >> 1) & 3) | ((tau >> 4) << 2)));
  auto sbase = [&](int l) AI -> unsigned {
    const int nn = l & 31, hh = l >> 5;
    return lds0 + (16 * (nn >> 4) + 8 * (nn & 1) + ((nn >> 1) & 7)) * 128 + 16 * ((2 * X + hh) ^ (((nn >> 2) & 3) | ((nn & 1) << 2)));
  };
  unsigned aw[4];
  {
    const unsigned b = GT ? lds0 + (16 * X + 8 * h + (t16 >> 2)) * 128 + 32 * ((2 * cg + ((lane >> 4) & 1)) ^ ((t16 >> 3) << 1)) + 8 * (t16 & 3)
                          : sbase(lane) + cg * 4096;
#pragma unroll
    for (int j = 0; j < 4; ++j) aw[j] = b ^ (GT ? j << 11 : j << 5);
  }
  auto flip = [&]() AI {
#pragma unroll
    for (int j = 0; j < 4; ++j) aw[j] ^= 65536;
    asm volatile("" : "+v"(aw[0]), "+v"(aw[1]), "+v"(aw[2]), "+v"(aw[3]));
  };
  auto wfrag = [&](unsigned a) AI -> bf16x8 {
    if (GT) return trfrag(a, 512);
    return *LP(bf16x8, a);
  };
  auto mm = [&](bf16x8 wfv, bf16x8 xfv, f32x16 c) AI -> f32x16 { return GT ? MFMA32(wfv, xfv, c) : MFMA32(xfv, wfv, c); };

  const f32x4 zero4 = {0.f, 0.f, 0.f, 0.f};
  f32x16 zero16;
#pragma unroll
  for (int i = 0; i < 16; ++i) zero16[i] = 0.f;
  f32x16 acc[4], accs, gr = zero16;
  f32x4 sm = zero4, csa = zero4;
  acc[0] = zero16;
  bf16x8 xf[32];
  bf16x8 wd2, wd3;
  auto p1tail = [&](int i) AI {
    gr = MFMA32(xf[4 * i + 2], xf[4 * i + 2], gr);
    sm = MFMA16(sel, xf[4 * i + 2], sm);
    acc[0] = mm(wd2, xf[4 * i + 2], acc[0]);
    acc[0] = mm(wd3, xf[4 * i + 3], acc[0]);
  };
  sfor<0, 8>([&](auto ic) AI {
    constexpr int i = decltype(ic)::value;
    RBAR(i, k1_wait(GT, i));
    if (i == 0) STAMP(2);
    if (i == 4) STAMP(3);
    constexpr int so = (i & 1) * 32768;
    const unsigned ax = axb + (i & 2) * 32768;
    xf[4 * i] = *LP(bf16x8, ax + so);
    const bf16x8 w0 = wfrag(aw[0] + so + 16384);
    xf[4 * i + 1] = *LP(bf16x8, (ax ^ 32) + so);
    const bf16x8 w1 = wfrag(aw[1] + so + 16384);
    xf[4 * i + 2] = *LP(bf16x8, (ax ^ 64) + so);
    const bf16x8 w2 = wfrag(aw[2] + so + 16384);
    xf[4 * i + 3] = *LP(bf16x8, (ax ^ 96) + so);
    const bf16x8 w3 = wfrag(aw[3] + so + 16384);
    if (i & 1) flip();
    SCHED();
    if (i > 0) { gr = MFMA32(xf[4 * i - 2], xf[4 * i - 2], gr); SCHED(); }
    issue(IC<i + 3>{}, 0);
    SCHED();
    if (i > 0) {
      acc[0] = mm(wd2, xf[4 * i - 2], acc[0]);
      sm = MFMA16(sel, xf[4 * i - 2], sm);
      acc[0] = mm(wd3, xf[4 * i - 1], acc[0]);
      SCHED();
    }
    issue(IC<i + 3>{}, 1);
    SCHED();
    gr = MFMA32(xf[4 * i], xf[4 * i], gr);
    sm = MFMA16(sel, xf[4 * i], sm);
    acc[0] = mm(w0, xf[4 * i], acc[0]);
    SCHED();
    issue(IC<i + 3>{}, 2);
    SCHED();
    csa = MFMA16(sel, w0, csa);
    acc[0] = mm(w1, xf[4 * i + 1], acc[0]);
    SCHED();
    if (i + 3 >= 8) issue(IC<i + 3>{}, 3);
    if (i < 4) param(i);
    wd2 = w2; wd3 = w3;
    SCHED();
  });

  float sdv, rsg;
  float dgt[4];
  f32x2 stp;
  f32x4 rq[2];
  f32x2 acv;
  float zc[2], zk[2], tg, csr;
  unsigned ob[8];
  f32x4 pv[2], stq;
  f32x2 cs2;
  u32x4 mB, cA;
  unsigned acs = lds0 + L1_CS + cg * 2560 + n * 16 + h * 8, arq = lds0 + L1_STF + w * 128 + h * 64;
  asm volatile("" : "+v"(acs), "+v"(arq));
  auto cs_put = [&](int p) AI {
    unsigned a = acs;
    asm volatile("" : "+v"(a));
    if (LLO) *LP(float, a + X * 4 + p * 512) = csa[0];
  };
  auto cs_get = [&](int p) AI { cs2 = *LP(f32x2, acs + p * 512); };
  auto cs_split = [&](int k) AI {
    if (k == 0) {
      const float c = cs2[0] + cs2[1];
      const unsigned c0 = __builtin_bit_cast(unsigned, c) & 0xffff0000u;
      csr = c - __builtin_bit_cast(float, c0);
      cA[0] = c0;
      cA[3] = 0u;
    } else {
      const unsigned c1 = __builtin_bit_cast(unsigned, csr) & 0xffff0000u;
      const float r2 = csr - __builtin_bit_cast(float, c1);
      cA[1] = c1;
      cA[2] = __builtin_bit_cast(unsigned, r2) & 0xffff0000u;
    }
    asm volatile("" : "+v"(cA));
  };
  auto corr = [&](f32x16& a, bool ssm) AI {
    const bf16x8 m = __builtin_bit_cast(bf16x8, mB), c = __builtin_bit_cast(bf16x8, cA);
    a = ssm ? MFMA32(m, c, a) : MFMA32(c, m, a);
  };
  auto lsel = [&](float a, float b, unsigned long long m) AI -> float {
    float d;
    asm volatile("v_cndmask_b32_e64 %0, %1, %2, %3" : "=v"(d) : "v"(a), "v"(b), "s"(m));
    return d;
  };
  auto stat_q = [&](int k) AI {
    const int l = olane();
    if (k < 4) {
      const float lo = lsel(gr[4 * k], gr[4 * k + 1], 0xAAAAAAAAAAAAAAAAull), hi = lsel(gr[4 * k + 2], gr[4 * k + 3], 0xAAAAAAAAAAAAAAAAull);
      dgt[k] = lsel(lo, hi, 0xCCCCCCCCCCCCCCCCull);
    } else if (k == 4) {
      const float lo = lsel(dgt[0], dgt[1], 0xFF00FF00FF00FF00ull), hi = lsel(dgt[2], dgt[3], 0xFF00FF00FF00FF00ull);
      dgt[0] = lsel(lo, hi, 0xFFFF0000FFFF0000ull);
    } else if (k == 5) {
      const unsigned a = lds0 + L1_ST + (32 * rg + tau) * 16 + cg * 8;
      if (LMASK(0xF0F0F0F00F0F0F0Full)) *LP(float, a) = dgt[0];
      if (LLO) *LP(float, a + 4) = sm[0];
    } else if (k == 6) {
      cs_put(0);
    } else if (k == 7) {
      if (!GT) *LP(f32x2, lds0 + L1_AC + (tsp * 32 + (l & 31)) * 8) = aco;
    } else if (k == 8) {
      stq = *LP(f32x4, lds0 + L1_ST + (32 * rg + tau) * 16);
    } else if (k == 9) {
      const float mean = (stq[1] + stq[3]) * (1.f / 512.f);
      const float var = fmaxf(fmaf(stq[0] + stq[2], 1.f / 512.f, -mean * mean), 0.f);
      stp = (f32x2){rbf(mean), var};
    } else if (k == 10) {
      sdv = rbf(__builtin_amdgcn_sqrtf(rbf(rbf(stp[1]) + 1e-5f)));
    } else if (k == 11) {
      const unsigned nm = __builtin_bit_cast(unsigned, -stp[0]);
      mB = (u32x4){nm, nm, nm, 0u};
      asm volatile("" : "+v"(mB));
    } else if (k == 12) {
      const float rs = __builtin_amdgcn_rcpf(sdv);
      rsg = rs * -1.44269504088896f;
      *LP(float, lds0 + L1_STF + w * 128 + rho * 4) = rs;
      if (nb == 0 && cg == 0 && l < 32) *(f32x2*)(ws + OFF_ST + (size_t)(pi * 128 + rg * 32 + tau) * 8) = (f32x2){stp[0], sdv};
    }
  };
  auto gate_q = [&](int p, int hq) AI {
    const int v = hq >> 1;
    if (!(hq & 1)) {
      tg = __builtin_amdgcn_exp2f(acc[p][v] * rsg);
    } else {
      float sv = __builtin_amdgcn_rcpf(fmaf(tg, sdv, sdv));
      asm volatile("" : "+v"(sv));
      acc[p][v] = sv;
    }
  };
  auto up_q = [&](int p, int k) AI {
    const int v = 2 * k;
    ob[k] = pk2(acc[p][v] * acc[p - 1][v], acc[p][v + 1] * acc[p - 1][v + 1]);
  };
  auto up_st = [&](int p, int e) AI {
    const u32x4 o = {ob[4 * e], ob[4 * e + 1], ob[4 * e + 2], ob[4 * e + 3]};
    const char* sb = uptr(ws, (unsigned)OFF_A + (((2 * pi + (rg >> 1)) * 112 + 8 * nb + 4 * (p >> 1) + 2 * cg + e) * 2 + (rg & 1)) * 1024);
    const unsigned vo = (tau + (olane() & 32)) * 16;
    GST16S(sb, vo, o);
  };
  auto ssm_pre = [&](int ts) AI { acv = GT ? aco : *LP(f32x2, lds0 + L1_AC + (ts * 32 + (olane() & 31)) * 8); };
  auto ssm_mr = [&](int c, int g) AI { rq[c] = *LP(f32x4, arq + (8 * c + 4 * g) * 4); };
  auto ssm_b = [&](float t, int c, int i, int o) AI {
    const float zo = zc[c];
    zc[c] = i == 0 ? rq[c][0] * t : fmaf(acv[0], zo, fmaf(rq[c][i & 3], t, SWP(zo) * acv[1]));
    if (i & 1) ob[o + 4 * c + (i >> 1)] = pk2(zo, zc[c]);
  };
  auto f_vst = [&](int l) AI -> unsigned { return ((l & 31) ^ cg ^ ((l >> 5) << 3)) * 16 + (l >> 5) * 1024; };
  auto f_vag = [&](int l) AI -> unsigned {
    return lds0 + L1_AGG + cg * 8192 + (l & 31) * 64 + ((rg ^ ((l >> 2) & 3)) * 16) + (2 * (l >> 5) + cg) * 4;
  };
  unsigned vst, vag;
  auto ssm_st = [&](int ts, int c) AI {
    const u32x4 o = {ob[4 * c], ob[4 * c + 1], ob[4 * c + 2], ob[4 * c + 3]};
    const char* sb = uptr(ws, (unsigned)OFF_H + (((2 * pi + (rg >> 1)) * 32 + tb0 + ts) * 8 + 4 * (rg & 1) + (c ^ cg)) * 512);
    const unsigned vo = (GT ? f_vst(olane()) : vst) ^ (c * 16);
    GST16S(sb, vo, o);
  };
  auto agg_put = [&](int p, int c, float z) AI {
    const unsigned a = (GT ? f_vag(olane()) : vag) ^ (c * 4);
    if (p == 4) *LP(float, a + (16384 - cg * 8192)) = z;
    else *LP(float, a + p * 2048) = z;
  };
  auto ssm_q = [&](int p, int q) AI {
    const int c = q & 1, i = q >> 1;
    ssm_b(acc[p][8 * c + i], c, i, 0);
    if (i == 3) ssm_mr(c, 1);
    if (i == 7) {
      if (p == 0) zk[c] = zc[c];
      else agg_put(p, c, zc[c]);
      if (p < 3) ssm_st(4 * cg + p, c);
    }
  };
  const unsigned exs = lds0 + L1_AGG;
  auto ss_send = [&](int g) AI {
    *LP(f32x4, exs + w * 2048 + g * 1024 + olane() * 16) = (f32x4){accs[8 + 4 * g], accs[9 + 4 * g], accs[10 + 4 * g], accs[11 + 4 * g]};
  };
  auto ss_recv = [&]() AI {
    const unsigned a = exs + (w ^ 4) * 2048 + olane() * 16;
    pv[0] = *LP(f32x4, a);
    pv[1] = *LP(f32x4, a + 1024);
  };
  auto ss_q = [&](int i) AI {
    ssm_b(accs[i] + pv[i >> 2][i & 3], 0, i, 0);
    if (i == 3) ssm_mr(0, 1);
  };
  f32x4 pz[2];
  f32x2 pa, pb;
  float pcv;
  unsigned pla, pso, pto;
  auto pfx_pre = [&](int ts, int k) AI {
    const int l = olane();
    const unsigned cp = (tb0 + ts) * 32 + (l & 31);
    if (k == 0) {
      pa = GT ? aco : *LP(f32x2, lds0 + L1_AC + (ts * 32 + (l & 31)) * 8);
      pla = (lds0 + L1_AGG + ts * 2048 + (l & 31) * 64 + ((l >> 2) & 3) * 16) ^ (l & 32);
      asm volatile("" : "+v"(pla));
    } else if (k == 1) {
      pa = csq(csq(csq(pa)));
      asm volatile("" : "+v"(pa));
    } else if (k == 2) {
      pb = csq(csq(csq(pa)));
      asm volatile("" : "+v"(pb));
    } else {
      pso = (unsigned)OFF_CAR + pi * 65536 + (l >> 5) * 32768 + cp * 16;
      pto = LLO ? (unsigned)OFF_T0 + (pi * 1024 + cp) * 4 : (unsigned)OFF_TOT + (cp * 16 + pi) * 4;
      asm volatile("" : "+v"(pso), "+v"(pto));
    }
  };
  auto pfx_run = [&]() AI {
    pz[0] = *LP(f32x4, pla);
    pz[1] = *LP(f32x4, pla ^ 16);
#pragma unroll
    for (int s = 0; s < 8; ++s) {
      const float z = pz[s >> 2][s & 3];
      pz[s >> 2][s & 3] = s == 0 ? 0.f : pcv;
      pcv = s == 0 ? z : fmaf(pa[0], pcv, z) + SWP(pcv) * pa[1];
    }
    *(f32x4*)(ws + pso) = (f32x4){pz[0][0], pz[1][0], pz[0][1], pz[1][1]};
    *(f32x4*)(ws + 16384 + pso) = (f32x4){pz[0][2], pz[1][2], pz[0][3], pz[1][3]};
    float t0 = pcv, t1 = pcv;
    asm volatile("s_nop 1\n\tv_permlane32_swap_b32 %0, %1" : "+v"(t0), "+v"(t1));
    const float tot = fmaf(pb[0], t0, pcv) + SWP(t0) * pb[1];
    *(float*)(ws + pto) = LLO ? pcv : tot;
  };
  auto quantum = [&](auto uc) AI {
    constexpr int u = decltype(uc)::value;
    if constexpr (u >= 1 && u < 9) stat_q(u - 1);
    if constexpr (u == 13) { stat_q(8); cs_get(0); }
    if constexpr (u >= 15 && u < 19) stat_q(u - 6);
    if constexpr (u == 45 || u == 77 || u == 104) cs_get((u - 8) / 32);
    if constexpr (u == 14 || u == 46 || u == 78 || u == 106) cs_split(0);
    if constexpr (u == 17 || u == 47 || u == 79 || u == 107) cs_split(1);
    if constexpr (u == 17) corr(acc[0], !GT);
    if constexpr (u == 47 || u == 79 || u == 107) corr(acc[(u - 11) / 32], !GT);
    if constexpr (u == 19 || u == 21) ss_send((u - 19) / 2);
    if constexpr (u == 66 || u == 98) {
      if (w == PFW) {
        if (u == 66) { pfx_pre(8, 0); pfx_pre(8, 3); pfx_pre(8, 1); pfx_pre(8, 2); }
        else pfx_run();
      }
    }
    if constexpr (GT) {
      if constexpr (u >= 20 && u < 52) gate_q(0, u - 20);
      if constexpr (u >= 49 && u < 57) up_q(1, u - 49);
      if constexpr (u == 53) up_st(1, 0);
      if constexpr (u == 54) cs_get(4);
      if constexpr (u == 55 || u == 56) cs_split(u - 55);
      if constexpr (u == 56) corr(accs, true);
      if constexpr (u == 57) { up_st(1, 1); ssm_pre(8); ss_recv(); ssm_mr(0, 0); }
      if constexpr (u >= 59 && u < 75 && (u & 1)) ss_q((u - 59) / 2);
      if constexpr (u == 75) agg_put(4, 0, zc[0]);
      if constexpr (u == 100) ssm_st(8, 0);
      if constexpr (u >= 81) gate_q(2, u - 81);
    } else {
      if constexpr (u == 30) cs_get(4);
      if constexpr (u == 31 || u == 32) cs_split(u - 31);
      if constexpr (u == 32) corr(accs, true);
      if constexpr (u == 41) ss_recv();
      if constexpr (u == 43) { ssm_pre(8); ssm_mr(0, 0); }
      if constexpr (u == 48 || u == 50) agg_put(0, (u - 48) / 2, zk[(u - 48) / 2]);
      sfor<0, 16>([&](auto gc) AI {
        constexpr int i = decltype(gc)::value;
        if constexpr (i < 8 && u == k1_s_unit(4, i)) ss_q(i);
        if constexpr (i == 7 && u == k1_s_unit(4, 7)) { ssm_st(8, 0); agg_put(4, 0, zc[0]); ssm_pre(4 * cg + 1); ssm_mr(0, 0); ssm_mr(1, 0); }
      });
      sfor<0, 16>([&](auto gc) AI {
        constexpr int i = decltype(gc)::value;
        if constexpr (u == k1_s_unit(0, i)) ssm_q(0, i);
        if constexpr (u == k1_s_unit(1, i) + 2) ssm_q(1, i);
        if constexpr (u == k1_s_unit(2, i)) ssm_q(2, i);
      });
      if constexpr (u == 80 || u == 108) { ssm_pre(4 * cg + u / 36); ssm_mr(0, 0); ssm_mr(1, 0); }
      if constexpr (u == 105 || u >= 109) pfx_pre(w, u == 105 ? 0 : u - 108);
      if constexpr (u == 10) { vst = f_vst(olane()); asm volatile("" : "+v"(vst)); }
      if constexpr (u == 11) { vag = f_vag(olane()); asm volatile("" : "+v"(vag)); }
      if constexpr (u == 18) { ssm_pre(4 * cg); ssm_mr(0, 0); ssm_mr(1, 0); }
    }
  };

  unsigned as0, as2;
  bf16x8 f[4];
  auto rd = [&](auto uc) AI {
    constexpr int u = decltype(uc)::value;
    if constexpr (u < 112) {
      constexpr int s = 8 + (u >> 4), k = u & 15, so = (s & 1) * 32768;
      if constexpr (s == SSLOT) {
        if (k == 0) {
          as0 = sbase(olane()) + (s & 2) * 32768;
          as2 = as0 ^ 64;
        }
        f[u & 3] = *LP(bf16x8, ((k & 1) ? as2 : as0) + so + (k >> 1) * 4096);
      } else {
        f[u & 3] = wfrag(aw[k >> 2] + so + (k & 3) * 8192);
      }
      if ((s & 1) && k == 15 && s < 14) flip();
    }
  };
  RBAR(8, k1_wait(GT, 8));
  STAMP(4);
  rd(IC<0>{}); rd(IC<1>{}); rd(IC<2>{}); rd(IC<3>{});
  SCHED();
  p1tail(7);
  f32x4 csb = zero4;
  SCHED();
  sfor<0, 112>([&](auto uc) AI {
    constexpr int u = decltype(uc)::value;
    constexpr int s = 8 + (u >> 4), k = u & 15;
    constexpr bool ss = s == SSLOT;
    constexpr int sp = s - 9, p = 1 + (sp >> 1), half = sp & 1;
    constexpr bool first = k == 0 && (ss || half == 0);
    if constexpr (ss) {
      if (!(k & 1)) csb = MFMA16(sel, f[u & 3], first ? zero4 : csb);
      accs = MFMA32(xf[2 * k], f[u & 3], first ? zero16 : accs);
    } else {
      if (k < 4) csb = MFMA16(sel, f[u & 3], first ? zero4 : csb);
      acc[p] = mm(f[u & 3], xf[16 * half + 4 * (k & 3) + (k >> 2)], first ? zero16 : acc[p]);
    }
    SCHED();
    if constexpr (!ss && half == 1 && k == 6) { csa = csb; cs_put(p); }
    if constexpr (ss && k == 15) { csa = csb; cs_put(4); }
    if constexpr (k == 13 && s < 14) {
      if (s == 10) STAMP(11);
      if (s == 12) STAMP(12);
      RBAR(s + 1, k1_wait(GT, s + 1));
      STAMP(s - 3);
      rd(IC<u + 3>{}); rd(IC<u + 4>{});
    } else if constexpr (k != 12 || s == 14) {
      rd(IC<u + 4>{});
    }
    if constexpr (u == 104) { BARRIER(); }
    constexpr int t = k1_ival(u);
    if constexpr (t + 3 < 15) {
#pragma unroll
      for (int d = 0; d < 4; ++d)
        if (u == k1_dma_unit(t, d)) issue(IC<t + 3>{}, d);
    }
#pragma unroll
    for (int i = 0; i < K1_NPI; ++i)
      if (GT && u == k1_pf_unit(i)) prefetch(i);
    if constexpr (GT && u >= PT0 && u < PT0 + 3) ptouch(u - PT0);
    quantum(uc);
    SCHED();
  });
  STAMP(13);
  if (GT) {
    gate_q(2, 31);
#pragma unroll
    for (int k = 0; k < 8; ++k) {
      up_q(3, k);
      if (k == 3) up_st(3, 0);
    }
    up_st(3, 1);
  } else {
#pragma unroll
    for (int q = 0; q < 16; ++q) ssm_q(3, q);
    ssm_st(4 * cg + 3, 0);
    ssm_st(4 * cg + 3, 1);
    BARRIER();
    pfx_run();
  }
  STAMP(14);
  VMCNT(0);
  RT1(0, (pi * 16 + nb) * 8 + w, ws);
}

__global__ __launch_bounds__(512, 2) NOPK void k1(K1Args A) {
  extern __shared__ __attribute__((aligned(16))) char smem[];
  const int b = blockIdx.x;
  const int xcd = b & 7, r = b >> 3;
  const int pl = r & 7, nl = r >> 3;
  const int q = (xcd >> 1) & 1, kk = xcd >> 2;
  const int pi = (xcd & 1) * 8 + pl, nb = nl < 3 ? 7 * kk + 4 * q + nl : ((pl >> 2) == q ? 7 * kk + 3 : 14 + kk);
  const int c0 = (pl + 2 * nl) & 7;
  unsigned long long cpc;
  unsigned clen;
  asm volatile("s_getpc_b64 %0\nk1_pc_ref:\n\ts_mov_b32 %1, k2_code_end-k1_pc_ref" : "=s"(cpc), "=s"(clen));
  clen = clen < 65536u ? clen : 0u;
  if (nb < 14) k1_body<true>(A, smem, pi, nb, c0, r, cpc, clen);
  else k1_body<false>(A, smem, pi, nb, c0, r, cpc, clen);
}

struct K2Args {
  const u16 *x, *Dk, *wd, *Cre, *Cim;
  char* ws;
  float* out;
};
constexpr int L2_EXS = 131072;
constexpr int L2_EXY = 65536;
constexpr int L2_X = L2_EXS + 16384;
constexpr int L2_SIZE = L2_X + 10240;
#define AY 0
#define XT 6
#define YSC " sc1"
template <int S> __device__ __forceinline__ void dmac(const char* sbase, unsigned voff, unsigned lds_dst) {
  asm volatile("s_mov_b32 m0, %2\n\ts_nop 0\n\tglobal_load_lds_dwordx4 %0, %1" YSC " ; RING %3" ::"v"(voff), "s"(sbase), "s"(lds_dst), "i"(S) : "memory");
}
#define SBAR() do { asm volatile("" ::: "memory"); __builtin_amdgcn_s_barrier(); asm volatile("" ::: "memory"); } while (0)
#define K2ST(k, i)
#define K2OUT(k, w)
#define K2DECL
__device__ __forceinline__ void k2_load(const K2Args& A, unsigned lds0, int w, int ml, int mi, int nj) {
  const int lane = threadIdx.x & 63;
  K2DECL;
  K2ST(3, 0);
  char* ws = A.ws;
  const int r8 = lane >> 3, pc = lane & 7;
  const int rotg = (7 * ml) >> 1;
  const unsigned va = w * 2048 + lane * 16;
  const unsigned vw = (16 * w + r8) * 1024 + 16 * (pc ^ (((r8 >> 1) & 1) << 2));
  const unsigned vc = (8 * w + r8) * 1024 + 16 * (pc ^ ((4 * (w & 1) + (r8 >> 1)) & 7));
  auto issue = [&](auto sc) AI {
    constexpr int s = decltype(sc)::value;
    const unsigned sl = lds0 + (s & 3) * 32768;
    if constexpr (s >= 22) {
    } else if constexpr (s < 14) {
      const int gs = s + rotg >= 14 ? s + rotg - 14 : s + rotg;
      const char* ab = ws + OFF_A + (size_t)(mi * 14 + gs) * 16384;
      const char* wb = (const char*)A.wd + (size_t)gs * 131072 + nj * 128;
      dmar<s>(ab, va, sl + w * 2048);
      dmar<s>(ab + 1024, va, sl + w * 2048 + 1024);
      dmar<s>(wb, vw, sl + 16384 + w * 2048);
      dmar<s>(wb + 8192, vw, sl + 16384 + w * 2048 + 1024);
    } else {
      const int ys = (s - 14 + nj) & 7;
      const char* hb = ws + OFF_HF + (size_t)(mi * 8 + ys) * 16384;
      dmac<s>(hb, va, sl + w * 2048);
      dmac<s>(hb + 1024, va, sl + w * 2048 + 1024);
      dmar<s>((const char*)A.Cre + nj * 65536 + ys * 128, vc, sl + 16384 + w * 1024);
      dmar<s>((const char*)A.Cim + nj * 65536 + ys * 128, vc, sl + 24576 + w * 1024);
    }
  };
  {
    const int s0 = w >> 2;
    const int gs = s0 + rotg;
    const unsigned va0 = lane * 16, vw0 = r8 * 1024 + 16 * (pc ^ (((r8 >> 1) & 1) << 2));
    const char* ab = ws + OFF_A + (size_t)(mi * 14 + gs) * 16384 + (w & 3) * 4096;
    const char* wb = (const char*)A.wd + (size_t)gs * 131072 + nj * 128 + (w & 3) * 32768;
    const unsigned sl = lds0 + s0 * 32768 + (w & 3) * 4096;
#pragma unroll
    for (int d = 0; d < 4; ++d) {
      dma(ab + d * 1024, va0, sl + d * 1024);
      dma(wb + d * 8192, vw0, sl + 16384 + d * 1024);
    }
  }
  K2ST(3, 2);
  issue(IC<2>{});
  K2ST(3, 3);
  if (w < 4) VMCNT(4);
  SBAR();
  K2ST(3, 4);
  sfor<0, 21>([&](auto tc) AI {
    constexpr int t = decltype(tc)::value;
    if constexpr (t == XT) {
      dma((const char*)A.x + (size_t)mi * 65536 + nj * 128, (8 * w + r8) * 1024 + pc * 16, lds0 + L2_X + w * 1024);
      if (w == 0) dma(ws + OFF_ST + mi * 512, (lane & 31) * 16, lds0 + L2_X + 8192);
      if (w == 1) dma((const char*)A.Dk + nj * 128, pc * 16, lds0 + L2_X + 9216);
    }
    issue(IC<t + 3>{});
    RBAR(t + 1, (t + 2 < 22 ? 4 : 0) + (t + 3 < 22 ? 4 : 0));
    if (t & 1) K2ST(3, 5 + (t >> 1));
    if (t == 20) K2ST(3, 15);
  });
  K2OUT(3, w);
}

__device__ __forceinline__ float mulc(f32x2 c, float v) { return fmaf(c[0], v, SWP(v) * c[1]); }
__device__ __forceinline__ float fmac(f32x2 c, float v, float t) { return fmaf(SWP(v), c[1], fmaf(c[0], v, t)); }
__device__ __forceinline__ float bfadd(unsigned pair, unsigned selv, float c) {
  return __builtin_amdgcn_fdot2_f32_bf16(__builtin_bit_cast(bf16x2_t, pair), __builtin_bit_cast(bf16x2_t, selv), c, false);
}

struct K2Out {
  float sgv[8], xfv[8], rs;
  u32x2 xq[2], dq;
  f32x2 st[2];
  __device__ __forceinline__ void load(unsigned lds0, int w, int eh, int lane) {
    const int r = 32 * (w & 1) + 16 * eh + (lane >> 3);
    const unsigned co = (w >> 1) * 64 + (lane & 7) * 8;
#pragma unroll
    for (int e = 0; e < 2; ++e) {
      xq[e] = *LP(u32x2, lds0 + L2_X + (r + 8 * e) * 128 + co);
      st[e] = *LP(f32x2, lds0 + L2_X + 8192 + (r + 8 * e) * 8);
    }
    dq = *LP(u32x2, lds0 + L2_X + 9216 + co);
  }
  __device__ __forceinline__ void prep(int v) {
    const int e = v >> 2;
    if (!(v & 3)) rs = __builtin_amdgcn_rcpf(st[e][1]);
    const unsigned xw = xq[e][(v >> 1) & 1], dw = dq[(v >> 1) & 1];
    const float xx = bf2f((xw >> (16 * (v & 1))) & 0xffff), dd = bf2f((dw >> (16 * (v & 1))) & 0xffff);
    const float xn = rbf(rbf(xx - st[e][0]) * rs);
    xfv[v] = xx;
    sgv[v] = rbf(xn * dd);
    asm volatile("" : "+v"(xfv[v]), "+v"(sgv[v]));
  }
  __device__ __forceinline__ void finish(float* out, unsigned lds0, int w, int eh, int lane, int mi, int nj) {
    const int r8 = lane >> 3, pc = lane & 7;
    const unsigned exr = lds0 + w * 4096 + eh * 2048 + r8 * 128 + ((pc ^ r8) * 16);
    const unsigned eo = (64 * mi + 32 * (w & 1) + 16 * eh + r8) * D_ + 64 * nj + 32 * (w >> 1) + 4 * pc;
#pragma unroll
    for (int e = 0; e < 2; ++e) {
      const f32x4 y = *LP(f32x4, exr + L2_EXY + e * 1024), sv = *LP(f32x4, exr + L2_EXS + e * 1024);
      f32x4 o;
#pragma unroll
      for (int rr = 0; rr < 4; ++rr) o[rr] = fmaf(fmaf(2.f, y[rr], sgv[4 * e + rr]), sv[rr], xfv[4 * e + rr]);
      *(f32x4*)(out + eo + 8 * e * D_) = o;
    }
  }
};

__device__ __forceinline__ void k2_gate(const K2Args& A, unsigned lds0, int w, int mi, int nj) {
  const int lane = threadIdx.x & 63;
  char* ws = A.ws;
  K2DECL;
  K2ST(2, 0);
  RT0();
  const int wr = w & 1, wc = w >> 1;
  const int n = lane & 31, h = lane >> 5, t16 = lane & 15, g1 = (lane >> 4) & 1;
  const unsigned exw = lds0 + w * 4096 + n * 128 + ((h ^ (n & 7)) * 16);
  const unsigned arb = lds0 + wr * 1024 + lane * 16;
  const unsigned trw = lds0 + 16384 + (4 * h + (t16 >> 2)) * 128 + 32 * ((2 * wc + g1) ^ ((t16 >> 3) << 1)) + 8 * (t16 & 3);
  f32x16 acc;
#pragma unroll
  for (int i = 0; i < 16; ++i) acc[i] = 0.f;
  bf16x8 fa[4], fb[4];
  K2Out O;
  unsigned ga[2] = {arb, arb + 65536}, gb[2] = {trw, trw + 65536};
  asm volatile("" : "+v"(ga[1]), "+v"(gb[1]));
  auto rd = [&](auto uc) AI {
    constexpr int u = decltype(uc)::value, s = u >> 3, j = u & 7, so = (s & 1) * 32768, hf = (s >> 1) & 1;
    if constexpr (u < 112) {
      fa[u & 3] = *LP(bf16x8, ga[hf] + so + j * 2048);
      fb[u & 3] = trfrag(gb[hf] + so + j * 2048, 1024);
    }
  };
  SBAR();
  K2ST(2, 1);
  rd(IC<0>{}); rd(IC<1>{}); rd(IC<2>{});
  SCHED();
  sfor<0, 112>([&](auto uc) AI {
    constexpr int u = decltype(uc)::value;
    acc = MFMA32(fb[u & 3], fa[u & 3], acc);
    SCHED();
    if constexpr ((u & 7) == 5) BARRIER();
    rd(IC<u + 3>{});
    if constexpr (u == 74) O.load(lds0, w, 0, lane);
    if constexpr (u >= 78 && u < 110 && (u & 3) == 2) O.prep((u - 78) >> 2);
    SCHED();
    if ((u & 15) == 15) K2ST(2, 2 + (u >> 4));
  });
#pragma unroll
  for (int q = 0; q < 6; ++q) {
#pragma unroll
    for (int v = (16 * q) / 6; v < (16 * q + 16) / 6; ++v) {
      const float g = rbf(acc[v]);
      acc[v] = rbf(g * __builtin_amdgcn_rcpf(1.f + __expf(-g)));
    }
    asm volatile("" : "+v"(acc));
    SBAR();
  }
  SBAR();
#pragma unroll
  for (int g = 0; g < 4; ++g) {
    const f32x4 o = {acc[4 * g], acc[4 * g + 1], acc[4 * g + 2], acc[4 * g + 3]};
    const unsigned a = exw ^ (g << 5);
    asm volatile("ds_write_b128 %0, %1" ::"v"(a + L2_EXS), "v"(o) : "memory");
  }
  K2ST(2, 9);
  BARRIER();
  K2ST(2, 10);
  O.finish(A.out, lds0, w, 0, lane, mi, nj);
  K2ST(2, 11);
  RT1(1, blockIdx.x * 8 + w, ws);
  RT1(1, blockIdx.x * 8 + w + 4, ws);
}

__device__ __forceinline__ void k2_state(const K2Args& A, unsigned lds0, int w, int mi, int nj) {
  const int lane = threadIdx.x & 63, pi = mi >> 1;
  char* ws = A.ws;
  const int sg = lane >> 4;
  const unsigned cp = nj * 128 + 32 * w + (lane & 15);
  const unsigned hoff = w * 4096 + sg * 512 + (((lane & 15) ^ (sg & 1) ^ ((sg >> 1) << 3)) * 16);
  f32x2 a1[2], cr[2], fc[2];
  f32x4 tq[2], a4[2];
  float th[2];
  u32x4 hq[2][2];
  {
    const unsigned o0 = cp * 16, o1 = cp * 64 + (12 - 4 * sg) * 4, o2 = cp * 16 + (sg >> 1) * 16384 + (sg & 1) * 8;
    const char* b0 = ws + OFF_AT;
    const char* b0b = b0 + 256;
    GLD16S(a4[0], o0, b0); GLD16S(a4[1], o0, b0b);
#pragma unroll
    for (int j = 0; j < 2; ++j) {
      const char* b1 = ws + OFF_TOT + (pi - 16) * 4 + j * 1024;
      const char* b2 = ws + OFF_CAR + pi * 65536 + (mi & 1) * 32768 + j * 256;
      const char* b4 = ws + OFF_T0 + pi * 4096 + j * 64;
      GLD16S(tq[j], o1, b1); GLD8S(cr[j], o2, b2);
      asm volatile("global_load_dword %0, %1, %2" : "=v"(th[j]) : "v"(o0 >> 2), "s"(b4) : "memory");
    }
  }
  auto hload = [&](int j, int c) AI {
    const char* b3 = ws + OFF_H + (size_t)(mi * 8 + nj) * 16384 + j * 256 + c * 2048;
    GLD16S(hq[j][c], hoff, b3);
  };
  asm volatile("s_waitcnt vmcnt(6)" : "+v"(a4[0]), "+v"(a4[1])::"memory");
#pragma unroll
  for (int j = 0; j < 2; ++j) { a1[j] = (f32x2){a4[j][0], a4[j][1]}; fc[j] = (f32x2){a4[j][2], a4[j][3]}; }
  f32x2 a8[2], a16[2], a32[2], a64[2], aP[2], fF[2], fG[2];
#pragma unroll
  for (int j = 0; j < 2; ++j) {
    a8[j] = csq(csq(csq(a1[j]))); a16[j] = csq(a8[j]); a32[j] = csq(a16[j]); a64[j] = csq(a32[j]); aP[j] = csq(a64[j]);
  }
  asm volatile("" : "+v"(aP[0]), "+v"(aP[1]));
  SBAR();
  hload(0, 0); hload(0, 1);
  const f32x2 one = {1.f, 0.f};
#pragma unroll
  for (int j = 0; j < 2; ++j) {
    const f32x2 p4 = csq(csq(aP[j])), p8 = csq(p4);
    fF[j] = cmul(LB4 ? p4 : one, LLO ? one : p8);
    fG[j] = cmul(a1[j], cmul(LB4 ? a8[j] : one, LLO ? one : a16[j]));
  }
  asm volatile("" : "+v"(fF[0]), "+v"(fF[1]), "+v"(fG[0]), "+v"(fG[1]));
  SBAR();
  hload(1, 0); hload(1, 1);
  asm volatile("s_waitcnt vmcnt(4)" : "+v"(cr[0]), "+v"(cr[1]), "+v"(tq[0]), "+v"(tq[1]), "+v"(th[0]), "+v"(th[1])::"memory");
  unsigned slo = 0x00003F80u, shi = 0x3F800000u;
  asm volatile("" : "+s"(slo), "+s"(shi));
  float pr[2][2];
  {
    const int i0 = pi - 4 - 4 * sg;
    float hs[2], b[2];
#pragma unroll
    for (int j = 0; j < 2; ++j) {
      hs[j] = i0 >= 0 ? tq[j][0] : 0.f;
#pragma unroll
      for (int m = 1; m < 4; ++m) hs[j] = fmac(aP[j], hs[j], i0 + m >= 0 ? tq[j][m] : 0.f);
      hs[j] = mulc(fF[j], hs[j]);
      b[j] = hs[j];
    }
    asm volatile("s_nop 1\n\tv_permlane16_swap_b32 %0, %1\n\tv_permlane16_swap_b32 %2, %3" : "+v"(hs[0]), "+v"(b[0]), "+v"(hs[1]), "+v"(b[1]));
    hs[0] += b[0]; hs[1] += b[1];
    b[0] = hs[0]; b[1] = hs[1];
    asm volatile("s_nop 1\n\tv_permlane32_swap_b32 %0, %1\n\tv_permlane32_swap_b32 %2, %3" : "+v"(hs[0]), "+v"(b[0]), "+v"(hs[1]), "+v"(b[1]));
#pragma unroll
    for (int j = 0; j < 2; ++j) {
      const float se = hs[j] + b[j], g0 = mulc(fG[j], (mi & 1) ? fmac(a64[j], se, th[j]) : se), g1v = mulc(a32[j], g0);
      pr[j][0] = fmac(a1[j], cr[j][0], g0);
      pr[j][1] = fmac(a1[j], cr[j][1], g1v);
    }
    asm volatile("" : "+v"(pr[0][0]), "+v"(pr[0][1]), "+v"(pr[1][0]), "+v"(pr[1][1]));
  }
  SBAR();
#pragma unroll
  for (int j = 0; j < 2; ++j) {
    asm volatile("s_waitcnt vmcnt(2)" : "+v"(hq[j][0]), "+v"(hq[j][1])::"memory");
#pragma unroll
    for (int q = 0; q < 4; ++q) {
#pragma unroll
      for (int c = 0; c < 2; ++c) {
        const float p1 = mulc(a1[j], pr[j][c]);
        hq[j][c][q] = pk2(mulc(fc[j], bfadd(hq[j][c][q], slo, pr[j][c])), mulc(fc[j], bfadd(hq[j][c][q], shi, p1)));
        if (q < 3) pr[j][c] = mulc(a1[j], p1);
      }
    }
#pragma unroll
    for (int c = 0; c < 2; ++c) {
      const char* sb = ws + OFF_HF + (size_t)(mi * 8 + nj) * 16384 + j * 256 + c * 2048;
      GST16S(sb, hoff, hq[j][c]);
    }
    SBAR();
  }
  SBAR();
  SBAR();
  VMCNT(0);
  SBAR();
  const char* cb = ws + OFF_CNT + mi * 128;
  const unsigned vz = 0;
  unsigned cnt;
  if (w == 0 && lane == 0) asm volatile("global_atomic_add %0, %1, %2 sc1" ::"v"(vz), "v"(1u), "s"(cb) : "memory");
  SBAR();
  SBAR();
  if (w == 0) asm volatile("global_load_dword %0, %1, %2 sc1" : "=v"(cnt) : "v"(vz), "s"(cb) : "memory");
  const int wr = w & 1, wc = w >> 1;
  const int n = lane & 31, h = lane >> 5, t16 = lane & 15, g1 = (lane >> 4) & 1;
  const unsigned exw = lds0 + w * 4096 + n * 128 + ((h ^ (n & 7)) * 16);
  const unsigned trh = lds0 + (4 * wr + 2 * g1 + ((t16 >> 1) & 1)) * 512 + ((16 * h + 8 * g1 + 2 * (t16 >> 2) + ((t16 >> 1) & 1)) * 16) + (t16 & 1) * 8;
  const int cd = 32 * wc + n;
  const unsigned crb = lds0 + 16384 + cd * 128 + 16 * (h ^ ((cd >> 1) & 7));
  unsigned ya[2][4], yc[2][4];
#pragma unroll
  for (int i = 0; i < 4; ++i) {
    ya[0][i] = trh ^ ((i & 1) * 16) ^ ((i >> 1) * 128); ya[1][i] = ya[0][i] + 65536;
    yc[0][i] = crb ^ (i << 5); yc[1][i] = yc[0][i] + 65536;
  }
  asm volatile("" : "+v"(ya[1][0]), "+v"(ya[1][1]), "+v"(ya[1][2]), "+v"(ya[1][3]));
  asm volatile("" : "+v"(yc[1][0]), "+v"(yc[1][1]), "+v"(yc[1][2]), "+v"(yc[1][3]));
  SBAR();
  if (w == 0) {
    asm volatile("s_waitcnt vmcnt(0)" : "+v"(cnt)::"memory");
    while (__builtin_amdgcn_readfirstlane(cnt) < 8u) {
      __builtin_amdgcn_s_sleep(2);
      asm volatile("global_load_dword %0, %1, %2 sc1\n\ts_waitcnt vmcnt(0)" : "=v"(cnt) : "v"(vz), "s"(cb) : "memory");
    }
  }
  SBAR();
  K2Out O;
  O.load(lds0, w, 1, lane);
#pragma unroll
  for (int v = 0; v < 3; ++v) O.prep(v);
  SBAR();
#pragma unroll
  for (int v = 3; v < 6; ++v) O.prep(v);
  SBAR();
#pragma unroll
  for (int v = 6; v < 8; ++v) O.prep(v);
  f32x16 acc;
#pragma unroll
  for (int i = 0; i < 16; ++i) acc[i] = 0.f;
  bf16x8 fa[4], fb[4];
  auto rd = [&](auto uc) AI {
    constexpr int u = decltype(uc)::value, s = 14 + (u >> 3), i = (u >> 1) & 3, part = u & 1, so = (s & 1) * 32768, hf = (s >> 1) & 1;
    if constexpr (u < 64) {
      fa[u & 3] = trfrag2(ya[hf][part] + so + i * 4096, ya[hf][2 + part] + so + i * 4096);
      fb[u & 3] = *LP(bf16x8, yc[hf][i] + so + part * 8192);
    }
  };
  __builtin_amdgcn_s_setprio(2);
  SBAR();
  rd(IC<0>{}); rd(IC<1>{}); rd(IC<2>{});
  SCHED();
  sfor<0, 64>([&](auto uc) AI {
    constexpr int u = decltype(uc)::value;
    acc = MFMA32(fb[u & 3], fa[u & 3], acc);
    SCHED();
    if constexpr ((u & 7) == 5 && u < 56) BARRIER();
    rd(IC<u + 3>{});
    SCHED();
  });
#pragma unroll
  for (int g = 0; g < 4; ++g)
    *LP(f32x4, (exw ^ (g << 5)) + L2_EXY) = (f32x4){acc[4 * g], acc[4 * g + 1], acc[4 * g + 2], acc[4 * g + 3]};
  BARRIER();
  O.finish(A.out, lds0, w, 1, lane, mi, nj);
}

__global__ __launch_bounds__(1024, 4) NOPK void k2(K2Args A) {
  extern __shared__ __attribute__((aligned(16))) char smem[];
  const unsigned lds0 = (unsigned)(uintptr_t)(LAS char*)smem;
  const int w = __builtin_amdgcn_readfirstlane(threadIdx.x >> 6) & 15;
  const int b = blockIdx.x;
  const int xcd = b & 7, r = b >> 3;
  const int ml = r >> 3, mi = xcd * 4 + ml, nj = r & 7;
  if (w < 4) {
    k2_state(A, lds0, w, mi, nj);
  } else if (w < 12) {
    k2_load(A, lds0, w - 4, ml, mi, nj);
  } else {
    __builtin_amdgcn_s_setprio(1);
    k2_gate(A, lds0, w & 3, mi, nj);
  }
  asm volatile("k2_code_end:");
}

extern "C" void kernel_launch(void* const* d_in, const int* in_sizes, int n_in, void* d_out, int out_size, void* d_ws,
                              size_t ws_size, hipStream_t stream) {
  K1Args a;
  a.x = (const u16*)d_in[0]; a.Lre = (const u16*)d_in[1]; a.Lim = (const u16*)d_in[2];
  a.Bre = (const u16*)d_in[3]; a.Bim = (const u16*)d_in[4]; a.Cre = (const u16*)d_in[5]; a.Cim = (const u16*)d_in[6];
  a.ldt = (const u16*)d_in[7]; a.wg = (const u16*)d_in[10]; a.wu = (const u16*)d_in[11]; a.wd = (const u16*)d_in[12];
  a.ws = (char*)d_ws;
  (void)hipFuncSetAttribute((const void*)k1, hipFuncAttributeMaxDynamicSharedMemorySize, L1_SIZE);
  (void)hipFuncSetAttribute((const void*)k2, hipFuncAttributeMaxDynamicSharedMemorySize, L2_SIZE);
  k1<<<256, 512, L1_SIZE, stream>>>(a);
  K2Args b;
  b.x = (const u16*)d_in[0]; b.Dk = (const u16*)d_in[8]; b.wd = (const u16*)d_in[12];
  b.Cre = (const u16*)d_in[5]; b.Cim = (const u16*)d_in[6];
  b.ws = (char*)d_ws; b.out = (float*)d_out;
  k2<<<256, 1024, L2_SIZE, stream>>>(b);
}
```

```cpp
#include <hip/hip_runtime.h>
#include <hip/hip_bf16.h>
#include <stdint.h>

typedef unsigned short u16;
typedef __attribute__((ext_vector_type(8))) short bf16x8;
typedef __attribute__((ext_vector_type(4))) short s16x4;
typedef __attribute__((ext_vector_type(16))) float f32x16;
typedef __attribute__((ext_vector_type(4))) float f32x4;
typedef __attribute__((ext_vector_type(2))) float f32x2;
typedef __attribute__((ext_vector_type(4))) unsigned u32x4;
typedef __attribute__((ext_vector_type(2))) unsigned u32x2;
typedef __attribute__((ext_vector_type(2))) __bf16 bf16x2_t;
#define LAS __attribute__((address_space(3)))
#if defined(__HIP_DEVICE_COMPILE__)
#define NOPK __attribute__((target("no-packed-fp32-ops")))
#else
#define NOPK
#endif

constexpr int D_ = 512;

constexpr size_t OFF_A   = 0;
constexpr size_t OFF_H   = OFF_A + (size_t)32 * 112 * 2048;
constexpr size_t OFF_CAR = OFF_H + (size_t)32 * 32 * 4096;
constexpr size_t OFF_TOT = OFF_CAR + (size_t)16 * 1024 * 64;
constexpr size_t OFF_AT  = OFF_TOT + (size_t)16 * 4096;
constexpr size_t OFF_ST  = OFF_AT + 16384;
constexpr size_t OFF_CNT = OFF_ST + 16384;
constexpr size_t OFF_T0  = OFF_CNT + 4096;
constexpr size_t OFF_HF  = OFF_T0 + 65536;

constexpr int L1_AGG = 131072;
constexpr int L1_AC  = L1_AGG + 18432;
constexpr int L1_STF = L1_AC + 4608;
constexpr int L1_ST  = L1_STF + 1024;
constexpr int L1_CS  = L1_ST + 2048;
constexpr int L1_DUM = L1_CS + 5120;
constexpr int L1_SIZE = L1_DUM + 1024;

__device__ __forceinline__ float bf2f(unsigned h) { return __builtin_bit_cast(float, h << 16); }
__device__ __forceinline__ float rbf(float v) { return (float)(__bf16)v; }
__device__ __forceinline__ unsigned pk2(float lo, float hi) {
  bf16x2_t r = {(__bf16)lo, (__bf16)hi};
  return __builtin_bit_cast(unsigned, r);
}
template <int CTRL> __device__ __forceinline__ float dpp0(float x) {
  return __builtin_bit_cast(float, __builtin_amdgcn_update_dpp(0, __builtin_bit_cast(int, x), CTRL, 0xf, 0xf, true));
}
#define SWP(x) dpp0<0xB1>(x)
__device__ __forceinline__ f32x2 csq(f32x2 a) { return (f32x2){fmaf(a[0], a[0], -a[1] * a[1]), 2.f * a[0] * a[1]}; }
__device__ __forceinline__ f32x2 cmul(f32x2 a, f32x2 b) {
  return (f32x2){fmaf(a[0], b[0], -a[1] * b[1]), fmaf(a[0], b[1], a[1] * b[0])};
}
__device__ __forceinline__ f32x2 cfma(f32x2 a, f32x2 b, f32x2 c) {
  return (f32x2){fmaf(a[0], b[0], fmaf(-a[1], b[1], c[0])), fmaf(a[0], b[1], fmaf(a[1], b[0], c[1]))};
}
template <int V> struct IC { static constexpr int value = V; };
template <int I, int N, class F> __device__ __forceinline__ void sfor(F&& f) {
  if constexpr (I < N) { f(IC<I>{}); sfor<I + 1, N>(f); }
}
__device__ __forceinline__ const char* uptr(const void* base, unsigned off) {
  return (const char*)base + (size_t)(unsigned)__builtin_amdgcn_readfirstlane(off);
}
#define MFMA16(a, b, c) __builtin_amdgcn_mfma_f32_16x16x32_bf16(a, b, c, 0, 0, 0)
#define MFMA32(a, b, c) __builtin_amdgcn_mfma_f32_32x32x16_bf16(a, b, c, 0, 0, 0)
#define SCHED() __builtin_amdgcn_sched_barrier(0)
#define LP(T, a) ((LAS T*)(unsigned)(a))
#define AI __attribute__((always_inline))
#define LMASK(m) __builtin_amdgcn_inverse_ballot_w64(m)
#define LB0 LMASK(0xAAAAAAAAAAAAAAAAull)
#define LB1 LMASK(0xCCCCCCCCCCCCCCCCull)
#define LB3 LMASK(0xFF00FF00FF00FF00ull)
#define LB4 LMASK(0xFFFF0000FFFF0000ull)
#define LLO LMASK(0x00000000FFFFFFFFull)

#define GLD16(dst, p) asm volatile("global_load_dwordx4 %0, %1, off" : "=v"(dst) : "v"(p) : "memory")
#define GLD8(dst, p) asm volatile("global_load_dwordx2 %0, %1, off" : "=v"(dst) : "v"(p) : "memory")
#define GLD2(dst, p) asm volatile("global_load_ushort %0, %1, off" : "=v"(dst) : "v"(p) : "memory")
#define GLD16S(dst, vo, sb) asm volatile("global_load_dwordx4 %0, %1, %2" : "=v"(dst) : "v"(vo), "s"(sb) : "memory")
#define GLD8S(dst, vo, sb) asm volatile("global_load_dwordx2 %0, %1, %2" : "=v"(dst) : "v"(vo), "s"(sb) : "memory")
#define GST16S(sb, vo, v) asm volatile("global_store_dwordx4 %0, %1, %2 sc0 sc1\n\ts_nop 1" ::"v"(vo), "v"(v), "s"(sb) : "memory")
#define LDSW(a, off, v) asm volatile("ds_write_b128 %0, %1 offset:%2" ::"v"(a), "v"(v), "i"(off) : "memory")
#define VMCNT(n) asm volatile("s_waitcnt vmcnt(%0)" ::"i"(n) : "memory")
#define BARRIER() do { asm volatile("s_waitcnt lgkmcnt(0)" ::: "memory"); __builtin_amdgcn_s_barrier(); asm volatile("" ::: "memory"); } while (0)
#define RBAR(s, n) do { asm volatile("s_waitcnt vmcnt(%0) lgkmcnt(0) ; NEED %1" ::"i"(n), "i"(s) : "memory"); __builtin_amdgcn_s_barrier(); asm volatile("" ::: "memory"); } while (0)
__device__ __forceinline__ void dma(const char* sbase, unsigned voff, unsigned lds_dst) {
  asm volatile("s_mov_b32 m0, %2\n\ts_nop 0\n\tglobal_load_lds_dwordx4 %0, %1" ::"v"(voff), "s"(sbase), "s"(lds_dst) : "memory");
}
template <int S> __device__ __forceinline__ void dmar(const char* sbase, unsigned voff, unsigned lds_dst) {
  asm volatile("s_mov_b32 m0, %2\n\ts_nop 0\n\tglobal_load_lds_dwordx4 %0, %1 ; RING %3" ::"v"(voff), "s"(sbase), "s"(lds_dst), "i"(S) : "memory");
}
__device__ __forceinline__ bf16x8 trfrag2(unsigned a, unsigned b) {
  s16x4 lo = __builtin_amdgcn_ds_read_tr16_b64_v4i16(LP(s16x4, a));
  s16x4 hi = __builtin_amdgcn_ds_read_tr16_b64_v4i16(LP(s16x4, b));
  return __builtin_shufflevector(lo, hi, 0, 1, 2, 3, 4, 5, 6, 7);
}
__device__ __forceinline__ bf16x8 trfrag(unsigned a, int d) {
  s16x4 lo = __builtin_amdgcn_ds_read_tr16_b64_v4i16(LP(s16x4, a));
  s16x4 hi = __builtin_amdgcn_ds_read_tr16_b64_v4i16(LP(s16x4, a + d));
  return __builtin_shufflevector(lo, hi, 0, 1, 2, 3, 4, 5, 6, 7);
}

#define STAMP(i)
constexpr size_t OFF_DBG = (size_t)64 << 20;
#define RT0()
#define RT1(a,b,c)

struct K1Args {
  const u16 *x, *Lre, *Lim, *Bre, *Bim, *Cre, *Cim, *ldt, *wg, *wu, *wd;
  char* ws;
};

constexpr int k1_bar_unit(int s) { return 16 * (s - 8) - 3; }
constexpr int k1_ival(int u) { return 8 + (u + 3) / 16; }
constexpr int k1_nring(int s) { return s > 14 ? 0 : (s < 8 ? 3 : 4); }
constexpr int k1_dma_unit(int t, int d) { return t == 8 ? (d == 0 ? 1 : 4 * d - 2) : k1_bar_unit(t) + 1 + 4 * d; }
#define PFS 128
#define PT0 20
#define PF0 60
#define PFD 3
constexpr int K1_NPF = (90112 / (64 * PFS) + 7) / 8;
constexpr int K1_NPI = 13;
#define PNA 5
#define PA0 30
constexpr int k1_pf_unit(int i) { return i < PNA ? PA0 + PFD * i : PF0 + PFD * (i - PNA); }
constexpr int k1_s_unit(int p, int i) { return p == 0 ? 20 + (3 * i) / 2 : (p == 4 ? 45 + i : 22 + 30 * p + (3 * i) / 2); }
#define WUP 2
constexpr int k1_extra(bool gt, int u) {
  int n = 0;
  for (int i = 0; i < K1_NPI; ++i) n += gt && k1_pf_unit(i) == u;
  n += gt && u >= PT0 && u < PT0 + 3;
  if (gt) n += (u == 53) + (u == 57) + (u == 100);
  else n += (k1_s_unit(0, 14) == u) + (k1_s_unit(0, 15) == u) + (k1_s_unit(4, 7) == u) + (k1_s_unit(1, 14) + 2 == u) + (k1_s_unit(1, 15) + 2 == u) +
            (k1_s_unit(2, 14) == u) + (k1_s_unit(2, 15) == u);
  return n;
}
constexpr int k1_wait(bool gt, int s) {
  int n = k1_nring(s + 1) + k1_nring(s + 2) + (s == 0 ? 3 : 0) + (s <= WUP ? 1 : 0);
  for (int u = s >= 11 ? k1_dma_unit(s - 3, 3) : 0; u < k1_bar_unit(s); ++u) n += k1_extra(gt, u);
  return n;
}

#define PFW 7
template <bool GT>
__device__ __forceinline__ void k1_body(const K1Args& A, char* smem, int pi, int nb, int c0, int rx, unsigned long long cpc,
                                        unsigned clen) {
  const int tid = threadIdx.x, lane = tid & 63;
  const int w = __builtin_amdgcn_readfirstlane(tid >> 6) & 7;
  const int rg = w & 3, cg = w >> 2, X = rg ^ cg;
  STAMP(0);
  RT0();
  const unsigned lds0 = (unsigned)(uintptr_t)(LAS char*)smem;
  const int r8 = lane >> 3, pc = lane & 7;
  const int tb0 = GT ? nb - 8 : 14 + 9 * (nb - 14);
  constexpr int SSLOT = 8;
  auto olane = [&]() AI -> int { int l = lane; asm volatile("" : "+v"(l)); return l; };

  const char* xb = uptr(A.x, pi * 131072 + w * 16384);
  const unsigned vs = r8 * 1024 + 16 * (pc ^ ((r8 >> 1) | ((w & 1) << 2)));
  const unsigned vw = GT ? (8 * w + r8) * 3584 + 16 * (pc ^ (((r8 >> 1) & 1) << 2)) : vs;
  const char* bmat = uptr((w & 1) ? A.Bim : A.Bre, ((GT ? nb : tb0) * 16 + 8 * ((w >> 1) & 1)) * 1024);
  auto wbase = [&](int p, int c) AI -> const char* {
    if (GT) return (const char*)((p & 1) ? A.wu : A.wg) + (size_t)c * (64 * 3584) + (128 * nb + 64 * (p >> 1)) * 2;
    return bmat + (unsigned)((4 * cg + p) * 16384 + c * 128);
  };
  auto issue = [&](auto sc, int d) AI {
    constexpr int s = decltype(sc)::value;
    const unsigned dst = lds0 + (s & 3) * 32768;
    if constexpr (s < 8) {
      const int c = (c0 + s) & 7;
      if (d == 0) dmar<s>(xb + c * 128, vs, dst + w * 2048);
      if (d == 1) dmar<s>(wbase(0, c), vw, dst + 16384 + w * 1024);
      if (d == 2) dmar<s>(xb + c * 128 + 8192, vs, dst + w * 2048 + 1024);
    } else if constexpr (s == SSLOT) {
      const int i = 4 * cg + d;
      dmar<s>(uptr(bmat, (GT ? 0 : 8 * 16384) + ((c0 + i) & 7) * 128), vs, dst + i * 4096 + rg * 1024);
    } else if constexpr (s < 15) {
      constexpr int sp = s - 9;
      dmar<s>(wbase(1 + (sp >> 1), (c0 + 4 * (sp & 1) + d) & 7), vw, dst + d * 8192 + w * 1024);
    }
  };
  const int gx = rx < 24 ? rx : 24 + (rx & 3);
  auto prefetch = [&](int i) AI {
    const char* sb = i < 8 ? (const char*)A.wd + (gx + 28 * i) * 8192
                           : (i < 12 ? (const char*)(i < 10 ? A.Cre : A.Cim) + (gx + 28 * (i & 1)) * 8192
                                     : (const char*)(gx < 8 ? A.Cre : A.Cim) + (56 + (gx & 7)) * 8192);
    const unsigned vo = olane() * 128;
    asm volatile("s_mov_b32 m0, %2\n\ts_cmp_eq_u32 %3, %4\n\ts_cselect_b64 exec, -1, 0\n\tglobal_load_lds_dword %0, %1\n\ts_mov_b64 exec, -1"
                 ::"v"(vo), "s"(sb), "s"(lds0 + L1_DUM), "s"(i == 12 ? w + (gx & 16) : w), "i"(i & 7) : "memory", "scc");
  };
  auto ptouch = [&](int i) AI {
    const char* sb = (const char*)(i == 0 ? A.wd : (i == 1 ? A.Cre : A.Cim)) + gx * 8192;
    const unsigned vo = olane() * (28 * 8192);
    asm volatile("s_mov_b32 m0, %2\n\ts_cmp_eq_u32 %3, %4\n\ts_cselect_b64 exec, %5, 0\n\tglobal_load_lds_dword %0, %1\n\ts_mov_b64 exec, -1"
                 ::"v"(vo), "s"(sb), "s"(lds0 + L1_DUM), "s"(w), "i"(5 + i), "i"(i == 0 ? 0xff : 3) : "memory", "scc");
  };
  issue(IC<0>{}, 0); issue(IC<0>{}, 1); issue(IC<0>{}, 2);
  auto warm = [&]() AI {
    unsigned off = (rx & 15) * 4096 + lane * 64;
    off = off < clen ? off : 0;
    asm volatile("s_mov_b32 m0, %2\n\ts_cmp_eq_u32 %3, 7\n\ts_cselect_b64 exec, -1, 0\n\tglobal_load_lds_dword %0, %1\n\ts_mov_b64 exec, -1"
                 ::"v"(off), "s"(cpc), "s"(lds0 + L1_DUM), "s"(w) : "memory", "scc");
  };
  if (WUP == 0) warm();
  unsigned plr, pli, pdt;
  const int tsp = GT ? 8 : ((tid >> 5) < 8 ? (tid >> 5) : 8);
  {
    const int ch = 16 * (tb0 + tsp) + ((tid & 31) >> 1);
    GLD2(plr, A.Lre + ch); GLD2(pli, A.Lim + ch); GLD2(pdt, A.ldt + ch);
  }
  __builtin_amdgcn_s_barrier();
  issue(IC<1>{}, 0); issue(IC<1>{}, 1); issue(IC<1>{}, 2);
  __builtin_amdgcn_s_barrier();
  issue(IC<2>{}, 0); issue(IC<2>{}, 1); issue(IC<2>{}, 2);
  if (WUP == 2) warm();
  STAMP(1);
  char* ws = A.ws;
  float pe, pr_, pxx;
  f32x2 aco;
  int pq;
  auto param = [&](int k) AI {
    const float lr = bf2f(plr), li = bf2f(pli);
    if (k == 0) {
      asm volatile("" : "+v"(plr), "+v"(pli), "+v"(pdt));
      pxx = rbf(expf(bf2f(pdt)));
      asm volatile("" : "+v"(pxx));
    } else if (k == 1) {
      pe = expf(pxx * lr);
      const float th = pxx * li;
      const float kk = __builtin_rintf(th * 0.15915494f);
      const float r = fmaf(th, 6.4206383e-09f, fmaf(th, 0.15915494f, -kk));
      const float q = __builtin_rintf(4.f * r);
      pq = (int)q;
      pr_ = fmaf(-0.25f, q, r) * 6.2831855f;
      asm volatile("" : "+v"(pe), "+v"(pr_), "+v"(pq));
    } else if (k == 2) {
      const float z = pr_ * pr_;
      const float sn = fmaf(pr_ * z, fmaf(z, fmaf(z, -1.9515296e-4f, 8.3321609e-3f), -1.6666655e-1f), pr_);
      const float cs = fmaf(z * z, fmaf(z, fmaf(z, 2.4433157e-5f, -1.3887316e-3f), 4.1666646e-2f), fmaf(z, -0.5f, 1.f));
      const float c1 = (pq & 1) ? sn : cs, s1 = (pq & 1) ? cs : sn;
      aco = (f32x2){pe * (((pq + 1) & 2) ? -c1 : c1), pe * ((pq & 2) ? -s1 : s1)};
      asm volatile("" : "+v"(aco));
    } else if (k == 3) {
      const bool im = tid & 1;
      if (pi == 0) {
        const float ar = aco[0], ai = aco[1], nr = ar - 1.f, den = lr * lr + li * li;
        const float fr = (nr * lr + ai * li) / den, fi = (ai * lr - nr * li) / den;
        const f32x4 ac = {ar, im ? ai : -ai, im ? -fr : fr, -fi};
        if (tid < (GT ? 32 : 288)) *(f32x4*)(ws + OFF_AT + (size_t)((tb0 + tsp) * 32 + (tid & 31)) * 16) = ac;
      }
      aco[1] = im ? aco[1] : -aco[1];
      asm volatile("" : "+v"(aco));
      if (pi == 1 && nb == 0 && tid < 32) *(unsigned*)(ws + OFF_CNT + tid * 128) = 0u;
    }
  };

  const int n = lane & 31, h = lane >> 5, t16 = lane & 15;
  const int rho = 16 * ((n >> 2) & 1) + 4 * (n >> 3) + (n & 3);
  const int tau = rho ^ (8 * cg);
  const unsigned selw = (t16 == 4 * ((lane >> 4) & 1)) ? 0x3F803F80u : 0u;
  const bf16x8 sel = __builtin_bit_cast(bf16x8, (u32x4){selw, selw, selw, selw});
  const unsigned axb = lds0 + (32 * rg + tau) * 128 + 16 * ((2 * X + h) ^ (((tau >> 1) & 3) | ((tau >> 4) << 2)));
  auto sbase = [&](int l) AI -> unsigned {
    const int nn = l & 31, hh = l >> 5;
    return lds0 + (16 * (nn >> 4) + 8 * (nn & 1) + ((nn >> 1) & 7)) * 128 + 16 * ((2 * X + hh) ^ (((nn >> 2) & 3) | ((nn & 1) << 2)));
  };
  unsigned aw[4];
  {
    const unsigned b = GT ? lds0 + (16 * X + 8 * h + (t16 >> 2)) * 128 + 32 * ((2 * cg + ((lane >> 4) & 1)) ^ ((t16 >> 3) << 1)) + 8 * (t16 & 3)
                          : sbase(lane) + cg * 4096;
#pragma unroll
    for (int j = 0; j < 4; ++j) aw[j] = b ^ (GT ? j << 11 : j << 5);
  }
  auto flip = [&]() AI {
#pragma unroll
    for (int j = 0; j < 4; ++j) aw[j] ^= 65536;
    asm volatile("" : "+v"(aw[0]), "+v"(aw[1]), "+v"(aw[2]), "+v"(aw[3]));
  };
  auto wfrag = [&](unsigned a) AI -> bf16x8 {
    if (GT) return trfrag(a, 512);
    return *LP(bf16x8, a);
  };
  auto mm = [&](bf16x8 wfv, bf16x8 xfv, f32x16 c) AI -> f32x16 { return GT ? MFMA32(wfv, xfv, c) : MFMA32(xfv, wfv, c); };

  const f32x4 zero4 = {0.f, 0.f, 0.f, 0.f};
  f32x16 zero16;
#pragma unroll
  for (int i = 0; i < 16; ++i) zero16[i] = 0.f;
  f32x16 acc[4], accs, gr = zero16;
  f32x4 sm = zero4, csa = zero4;
  acc[0] = zero16;
  bf16x8 xf[32];
  bf16x8 wd2, wd3;
  auto p1tail = [&](int i) AI {
    gr = MFMA32(xf[4 * i + 2], xf[4 * i + 2], gr);
    sm = MFMA16(sel, xf[4 * i + 2], sm);
    acc[0] = mm(wd2, xf[4 * i + 2], acc[0]);
    acc[0] = mm(wd3, xf[4 * i + 3], acc[0]);
  };
  sfor<0, 8>([&](auto ic) AI {
    constexpr int i = decltype(ic)::value;
    RBAR(i, k1_wait(GT, i));
    if (i == 0) STAMP(2);
    if (i == 4) STAMP(3);
    constexpr int so = (i & 1) * 32768;
    const unsigned ax = axb + (i & 2) * 32768;
    xf[4 * i] = *LP(bf16x8, ax + so);
    const bf16x8 w0 = wfrag(aw[0] + so + 16384);
    xf[4 * i + 1] = *LP(bf16x8, (ax ^ 32) + so);
    const bf16x8 w1 = wfrag(aw[1] + so + 16384);
    xf[4 * i + 2] = *LP(bf16x8, (ax ^ 64) + so);
    const bf16x8 w2 = wfrag(aw[2] + so + 16384);
    xf[4 * i + 3] = *LP(bf16x8, (ax ^ 96) + so);
    const bf16x8 w3 = wfrag(aw[3] + so + 16384);
    if (i & 1) flip();
    SCHED();
    if (i > 0) { gr = MFMA32(xf[4 * i - 2], xf[4 * i - 2], gr); SCHED(); }
    issue(IC<i + 3>{}, 0);
    SCHED();
    if (i > 0) {
      acc[0] = mm(wd2, xf[4 * i - 2], acc[0]);
      sm = MFMA16(sel, xf[4 * i - 2], sm);
      acc[0] = mm(wd3, xf[4 * i - 1], acc[0]);
      SCHED();
    }
    issue(IC<i + 3>{}, 1);
    SCHED();
    gr = MFMA32(xf[4 * i], xf[4 * i], gr);
    sm = MFMA16(sel, xf[4 * i], sm);
    acc[0] = mm(w0, xf[4 * i], acc[0]);
    SCHED();
    issue(IC<i + 3>{}, 2);
    SCHED();
    csa = MFMA16(sel, w0, csa);
    acc[0] = mm(w1, xf[4 * i + 1], acc[0]);
    SCHED();
    if (i + 3 >= 8) issue(IC<i + 3>{}, 3);
    if (i >= 1 && i < 5) param(i - 1);
    wd2 = w2; wd3 = w3;
    SCHED();
  });

  float sdv, rsg;
  float dgt[4];
  f32x2 stp;
  f32x4 rq[2];
  f32x2 acv;
  float zc[2], zk[2], tg, csr;
  unsigned ob[8];
  f32x4 pv[2], stq;
  f32x2 cs2;
  u32x4 mB, cA;
  unsigned acs = lds0 + L1_CS + cg * 2560 + n * 16 + h * 8, arq = lds0 + L1_STF + w * 128 + h * 64;
  asm volatile("" : "+v"(acs), "+v"(arq));
  auto cs_put = [&](int p) AI {
    unsigned a = acs;
    asm volatile("" : "+v"(a));
    if (LLO) *LP(float, a + X * 4 + p * 512) = csa[0];
  };
  auto cs_get = [&](int p) AI { cs2 = *LP(f32x2, acs + p * 512); };
  auto cs_split = [&](int k) AI {
    if (k == 0) {
      const float c = cs2[0] + cs2[1];
      const unsigned c0 = __builtin_bit_cast(unsigned, c) & 0xffff0000u;
      csr = c - __builtin_bit_cast(float, c0);
      cA[0] = c0;
      cA[3] = 0u;
    } else {
      const unsigned c1 = __builtin_bit_cast(unsigned, csr) & 0xffff0000u;
      const float r2 = csr - __builtin_bit_cast(float, c1);
      cA[1] = c1;
      cA[2] = __builtin_bit_cast(unsigned, r2) & 0xffff0000u;
    }
    asm volatile("" : "+v"(cA));
  };
  auto corr = [&](f32x16& a, bool ssm) AI {
    const bf16x8 m = __builtin_bit_cast(bf16x8, mB), c = __builtin_bit_cast(bf16x8, cA);
    a = ssm ? MFMA32(m, c, a) : MFMA32(c, m, a);
  };
  auto lsel = [&](float a, float b, unsigned long long m) AI -> float {
    float d;
    asm volatile("v_cndmask_b32_e64 %0, %1, %2, %3" : "=v"(d) : "v"(a), "v"(b), "s"(m));
    return d;
  };
  auto stat_q = [&](int k) AI {
    const int l = olane();
    if (k < 4) {
      const float lo = lsel(gr[4 * k], gr[4 * k + 1], 0xAAAAAAAAAAAAAAAAull), hi = lsel(gr[4 * k + 2], gr[4 * k + 3], 0xAAAAAAAAAAAAAAAAull);
      dgt[k] = lsel(lo, hi, 0xCCCCCCCCCCCCCCCCull);
    } else if (k == 4) {
      const float lo = lsel(dgt[0], dgt[1], 0xFF00FF00FF00FF00ull), hi = lsel(dgt[2], dgt[3], 0xFF00FF00FF00FF00ull);
      dgt[0] = lsel(lo, hi, 0xFFFF0000FFFF0000ull);
    } else if (k == 5) {
      const unsigned a = lds0 + L1_ST + (32 * rg + tau) * 16 + cg * 8;
      if (LMASK(0xF0F0F0F00F0F0F0Full)) *LP(float, a) = dgt[0];
      if (LLO) *LP(float, a + 4) = sm[0];
    } else if (k == 6) {
      cs_put(0);
    } else if (k == 7) {
      if (!GT) *LP(f32x2, lds0 + L1_AC + (tsp * 32 + (l & 31)) * 8) = aco;
    } else if (k == 8) {
      stq = *LP(f32x4, lds0 + L1_ST + (32 * rg + tau) * 16);
    } else if (k == 9) {
      const float mean = (stq[1] + stq[3]) * (1.f / 512.f);
      const float var = fmaxf(fmaf(stq[0] + stq[2], 1.f / 512.f, -mean * mean), 0.f);
      stp = (f32x2){rbf(mean), var};
    } else if (k == 10) {
      sdv = rbf(__builtin_amdgcn_sqrtf(rbf(rbf(stp[1]) + 1e-5f)));
    } else if (k == 11) {
      const unsigned nm = __builtin_bit_cast(unsigned, -stp[0]);
      mB = (u32x4){nm, nm, nm, 0u};
      asm volatile("" : "+v"(mB));
    } else if (k == 12) {
      const float rs = __builtin_amdgcn_rcpf(sdv);
      rsg = rs * -1.44269504088896f;
      *LP(float, lds0 + L1_STF + w * 128 + rho * 4) = rs;
      if (nb == 0 && cg == 0 && l < 32) *(f32x2*)(ws + OFF_ST + (size_t)(pi * 128 + rg * 32 + tau) * 8) = (f32x2){stp[0], sdv};
    }
  };
  auto gate_q = [&](int p, int hq) AI {
    const int v = hq >> 1;
    if (!(hq & 1)) {
      tg = __builtin_amdgcn_exp2f(acc[p][v] * rsg);
    } else {
      float sv = __builtin_amdgcn_rcpf(fmaf(tg, sdv, sdv));
      asm volatile("" : "+v"(sv));
      acc[p][v] = sv;
    }
  };
  auto up_q = [&](int p, int k) AI {
    const int v = 2 * k;
    ob[k] = pk2(acc[p][v] * acc[p - 1][v], acc[p][v + 1] * acc[p - 1][v + 1]);
  };
  auto up_st = [&](int p, int e) AI {
    const u32x4 o = {ob[4 * e], ob[4 * e + 1], ob[4 * e + 2], ob[4 * e + 3]};
    const char* sb = uptr(ws, (unsigned)OFF_A + (((2 * pi + (rg >> 1)) * 112 + 8 * nb + 4 * (p >> 1) + 2 * cg + e) * 2 + (rg & 1)) * 1024);
    const unsigned vo = (tau + (olane() & 32)) * 16;
    GST16S(sb, vo, o);
  };
  auto ssm_pre = [&](int ts) AI { acv = GT ? aco : *LP(f32x2, lds0 + L1_AC + (ts * 32 + (olane() & 31)) * 8); };
  auto ssm_mr = [&](int c, int g) AI { rq[c] = *LP(f32x4, arq + (8 * c + 4 * g) * 4); };
  auto ssm_b = [&](float t, int c, int i, int o) AI {
    const float zo = zc[c];
    zc[c] = i == 0 ? rq[c][0] * t : fmaf(acv[0], zo, fmaf(rq[c][i & 3], t, SWP(zo) * acv[1]));
    if (i & 1) ob[o + 4 * c + (i >> 1)] = pk2(zo, zc[c]);
  };
  auto f_vst = [&](int l) AI -> unsigned { return ((l & 31) ^ cg ^ ((l >> 5) << 3)) * 16 + (l >> 5) * 1024; };
  auto f_vag = [&](int l) AI -> unsigned {
    return lds0 + L1_AGG + cg * 8192 + (l & 31) * 64 + ((rg ^ ((l >> 2) & 3)) * 16) + (2 * (l >> 5) + cg) * 4;
  };
  unsigned vst, vag;
  auto ssm_st = [&](int ts, int c) AI {
    const u32x4 o = {ob[4 * c], ob[4 * c + 1], ob[4 * c + 2], ob[4 * c + 3]};
    const char* sb = uptr(ws, (unsigned)OFF_H + (((2 * pi + (rg >> 1)) * 32 + tb0 + ts) * 8 + 4 * (rg & 1) + (c ^ cg)) * 512);
    const unsigned vo = (GT ? f_vst(olane()) : vst) ^ (c * 16);
    GST16S(sb, vo, o);
  };
  auto agg_put = [&](int p, int c, float z) AI {
    const unsigned a = (GT ? f_vag(olane()) : vag) ^ (c * 4);
    if (p == 4) *LP(float, a + (16384 - cg * 8192)) = z;
    else *LP(float, a + p * 2048) = z;
  };
  auto ssm_q = [&](int p, int q) AI {
    const int c = q & 1, i = q >> 1;
    ssm_b(acc[p][8 * c + i], c, i, 0);
    if (i == 3) ssm_mr(c, 1);
    if (i == 7) {
      if (p == 0) zk[c] = zc[c];
      else agg_put(p, c, zc[c]);
      if (p < 3) ssm_st(4 * cg + p, c);
    }
  };
  const unsigned exs = lds0 + L1_AGG;
  auto ss_send = [&](int g) AI {
    *LP(f32x4, exs + w * 2048 + g * 1024 + olane() * 16) = (f32x4){accs[8 + 4 * g], accs[9 + 4 * g], accs[10 + 4 * g], accs[11 + 4 * g]};
  };
  auto ss_recv = [&]() AI {
    const unsigned a = exs + (w ^ 4) * 2048 + olane() * 16;
    pv[0] = *LP(f32x4, a);
    pv[1] = *LP(f32x4, a + 1024);
  };
  auto ss_q = [&](int i) AI {
    ssm_b(accs[i] + pv[i >> 2][i & 3], 0, i, 0);
    if (i == 3) ssm_mr(0, 1);
  };
  f32x4 pz[2];
  f32x2 pa, pb;
  float pcv;
  unsigned pla, pso, pto;
  auto pfx_pre = [&](int ts, int k) AI {
    const int l = olane();
    const unsigned cp = (tb0 + ts) * 32 + (l & 31);
    if (k == 0) {
      pa = GT ? aco : *LP(f32x2, lds0 + L1_AC + (ts * 32 + (l & 31)) * 8);
      pla = (lds0 + L1_AGG + ts * 2048 + (l & 31) * 64 + ((l >> 2) & 3) * 16) ^ (l & 32);
      asm volatile("" : "+v"(pla));
    } else if (k == 1) {
      pa = csq(csq(csq(pa)));
      asm volatile("" : "+v"(pa));
    } else if (k == 2) {
      pb = csq(csq(csq(pa)));
      asm volatile("" : "+v"(pb));
    } else {
      pso = (unsigned)OFF_CAR + pi * 65536 + (l >> 5) * 32768 + cp * 16;
      pto = LLO ? (unsigned)OFF_T0 + (pi * 1024 + cp) * 4 : (unsigned)OFF_TOT + (cp * 16 + pi) * 4;
      asm volatile("" : "+v"(pso), "+v"(pto));
    }
  };
  auto pfx_run = [&]() AI {
    pz[0] = *LP(f32x4, pla);
    pz[1] = *LP(f32x4, pla ^ 16);
#pragma unroll
    for (int s = 0; s < 8; ++s) {
      const float z = pz[s >> 2][s & 3];
      pz[s >> 2][s & 3] = s == 0 ? 0.f : pcv;
      pcv = s == 0 ? z : fmaf(pa[0], pcv, z) + SWP(pcv) * pa[1];
    }
    *(f32x4*)(ws + pso) = (f32x4){pz[0][0], pz[1][0], pz[0][1], pz[1][1]};
    *(f32x4*)(ws + 16384 + pso) = (f32x4){pz[0][2], pz[1][2], pz[0][3], pz[1][3]};
    float t0 = pcv, t1 = pcv;
    asm volatile("s_nop 1\n\tv_permlane32_swap_b32 %0, %1" : "+v"(t0), "+v"(t1));
    const float tot = fmaf(pb[0], t0, pcv) + SWP(t0) * pb[1];
    *(float*)(ws + pto) = LLO ? pcv : tot;
  };
  auto quantum = [&](auto uc) AI {
    constexpr int u = decltype(uc)::value;
    if constexpr (u >= 1 && u < 9) stat_q(u - 1);
    if constexpr (u == 13) { stat_q(8); cs_get(0); }
    if constexpr (u >= 15 && u < 19) stat_q(u - 6);
    if constexpr (u == 45 || u == 77 || u == 104) cs_get((u - 8) / 32);
    if constexpr (u == 14 || u == 46 || u == 78 || u == 106) cs_split(0);
    if constexpr (u == 17 || u == 47 || u == 79 || u == 107) cs_split(1);
    if constexpr (u == 17) corr(acc[0], !GT);
    if constexpr (u == 47 || u == 79 || u == 107) corr(acc[(u - 11) / 32], !GT);
    if constexpr (u == 19 || u == 21) ss_send((u - 19) / 2);
    if constexpr (u == 66 || u == 98) {
      if (w == PFW) {
        if (u == 66) { pfx_pre(8, 0); pfx_pre(8, 3); pfx_pre(8, 1); pfx_pre(8, 2); }
        else pfx_run();
      }
    }
    if constexpr (GT) {
      if constexpr (u >= 20 && u < 52) gate_q(0, u - 20);
      if constexpr (u >= 49 && u < 57) up_q(1, u - 49);
      if constexpr (u == 53) up_st(1, 0);
      if constexpr (u == 54) cs_get(4);
      if constexpr (u == 55 || u == 56) cs_split(u - 55);
      if constexpr (u == 56) corr(accs, true);
      if constexpr (u == 57) { up_st(1, 1); ssm_pre(8); ss_recv(); ssm_mr(0, 0); }
      if constexpr (u >= 59 && u < 75 && (u & 1)) ss_q((u - 59) / 2);
      if constexpr (u == 75) agg_put(4, 0, zc[0]);
      if constexpr (u == 100) ssm_st(8, 0);
      if constexpr (u >= 81) gate_q(2, u - 81);
    } else {
      if constexpr (u == 30) cs_get(4);
      if constexpr (u == 31 || u == 32) cs_split(u - 31);
      if constexpr (u == 32) corr(accs, true);
      if constexpr (u == 41) ss_recv();
      if constexpr (u == 43) { ssm_pre(8); ssm_mr(0, 0); }
      if constexpr (u == 48 || u == 50) agg_put(0, (u - 48) / 2, zk[(u - 48) / 2]);
      sfor<0, 16>([&](auto gc) AI {
        constexpr int i = decltype(gc)::value;
        if constexpr (i < 8 && u == k1_s_unit(4, i)) ss_q(i);
        if constexpr (i == 7 && u == k1_s_unit(4, 7)) { ssm_st(8, 0); agg_put(4, 0, zc[0]); ssm_pre(4 * cg + 1); ssm_mr(0, 0); ssm_mr(1, 0); }
      });
      sfor<0, 16>([&](auto gc) AI {
        constexpr int i = decltype(gc)::value;
        if constexpr (u == k1_s_unit(0, i)) ssm_q(0, i);
        if constexpr (u == k1_s_unit(1, i) + 2) ssm_q(1, i);
        if constexpr (u == k1_s_unit(2, i)) ssm_q(2, i);
      });
      if constexpr (u == 80 || u == 108) { ssm_pre(4 * cg + u / 36); ssm_mr(0, 0); ssm_mr(1, 0); }
      if constexpr (u == 105 || u >= 109) pfx_pre(w, u == 105 ? 0 : u - 108);
      if constexpr (u == 10) { vst = f_vst(olane()); asm volatile("" : "+v"(vst)); }
      if constexpr (u == 11) { vag = f_vag(olane()); asm volatile("" : "+v"(vag)); }
      if constexpr (u == 18) { ssm_pre(4 * cg); ssm_mr(0, 0); ssm_mr(1, 0); }
    }
  };

  unsigned as0, as2;
  bf16x8 f[4];
  auto rd = [&](auto uc) AI {
    constexpr int u = decltype(uc)::value;
    if constexpr (u < 112) {
      constexpr int s = 8 + (u >> 4), k = u & 15, so = (s & 1) * 32768;
      if constexpr (s == SSLOT) {
        if (k == 0) {
          as0 = sbase(olane()) + (s & 2) * 32768;
          as2 = as0 ^ 64;
        }
        f[u & 3] = *LP(bf16x8, ((k & 1) ? as2 : as0) + so + (k >> 1) * 4096);
      } else {
        f[u & 3] = wfrag(aw[k >> 2] + so + (k & 3) * 8192);
      }
      if ((s & 1) && k == 15 && s < 14) flip();
    }
  };
  RBAR(8, k1_wait(GT, 8));
  STAMP(4);
  rd(IC<0>{}); rd(IC<1>{}); rd(IC<2>{}); rd(IC<3>{});
  SCHED();
  p1tail(7);
  f32x4 csb = zero4;
  SCHED();
  sfor<0, 112>([&](auto uc) AI {
    constexpr int u = decltype(uc)::value;
    constexpr int s = 8 + (u >> 4), k = u & 15;
    constexpr bool ss = s == SSLOT;
    constexpr int sp = s - 9, p = 1 + (sp >> 1), half = sp & 1;
    constexpr bool first = k == 0 && (ss || half == 0);
    if constexpr (ss) {
      if (!(k & 1)) csb = MFMA16(sel, f[u & 3], first ? zero4 : csb);
      accs = MFMA32(xf[2 * k], f[u & 3], first ? zero16 : accs);
    } else {
      if (k < 4) csb = MFMA16(sel, f[u & 3], first ? zero4 : csb);
      acc[p] = mm(f[u & 3], xf[16 * half + 4 * (k & 3) + (k >> 2)], first ? zero16 : acc[p]);
    }
    SCHED();
    if constexpr (!ss && half == 1 && k == 6) { csa = csb; cs_put(p); }
    if constexpr (ss && k == 15) { csa = csb; cs_put(4); }
    if constexpr (k == 13 && s < 14) {
      if (s == 10) STAMP(11);
      if (s == 12) STAMP(12);
      RBAR(s + 1, k1_wait(GT, s + 1));
      STAMP(s - 3);
      rd(IC<u + 3>{}); rd(IC<u + 4>{});
    } else if constexpr (k != 12 || s == 14) {
      rd(IC<u + 4>{});
    }
    if constexpr (u == 104) { BARRIER(); }
    constexpr int t = k1_ival(u);
    if constexpr (t + 3 < 15) {
#pragma unroll
      for (int d = 0; d < 4; ++d)
        if (u == k1_dma_unit(t, d)) issue(IC<t + 3>{}, d);
    }
#pragma unroll
    for (int i = 0; i < K1_NPI; ++i)
      if (GT && u == k1_pf_unit(i)) prefetch(i);
    if constexpr (GT && u >= PT0 && u < PT0 + 3) ptouch(u - PT0);
    quantum(uc);
    SCHED();
  });
  STAMP(13);
  if (GT) {
    gate_q(2, 31);
#pragma unroll
    for (int k = 0; k < 8; ++k) {
      up_q(3, k);
      if (k == 3) up_st(3, 0);
    }
    up_st(3, 1);
  } else {
#pragma unroll
    for (int q = 0; q < 16; ++q) ssm_q(3, q);
    ssm_st(4 * cg + 3, 0);
    ssm_st(4 * cg + 3, 1);
    BARRIER();
    pfx_run();
  }
  STAMP(14);
  VMCNT(0);
  RT1(0, (pi * 16 + nb) * 8 + w, ws);
}

__global__ __launch_bounds__(512, 2) NOPK void k1(K1Args A) {
  extern __shared__ __attribute__((aligned(16))) char smem[];
  const int b = blockIdx.x;
  const int xcd = b & 7, r = b >> 3;
  const int pl = r & 7, nl = r >> 3;
  const int q = (xcd >> 1) & 1, kk = xcd >> 2;
  const int pi = (xcd & 1) * 8 + pl, nb = nl < 3 ? 7 * kk + 4 * q + nl : ((pl >> 2) == q ? 7 * kk + 3 : 14 + kk);
  const int c0 = (pl + 2 * nl) & 7;
  unsigned long long cpc;
  unsigned clen;
  asm volatile("s_getpc_b64 %0\nk1_pc_ref:\n\ts_mov_b32 %1, k2_code_end-k1_pc_ref" : "=s"(cpc), "=s"(clen));
  clen = clen < 65536u ? clen : 0u;
  if (nb < 14) k1_body<true>(A, smem, pi, nb, c0, r, cpc, clen);
  else k1_body<false>(A, smem, pi, nb, c0, r, cpc, clen);
}

struct K2Args {
  const u16 *x, *Dk, *wd, *Cre, *Cim;
  char* ws;
  float* out;
};
constexpr int L2_EXS = 131072;
constexpr int L2_EXY = 65536;
constexpr int L2_X = L2_EXS + 16384;
constexpr int L2_SIZE = L2_X + 10240;
#define AY 0
#define XT 6
#define YSC " sc1"
template <int S> __device__ __forceinline__ void dmac(const char* sbase, unsigned voff, unsigned lds_dst) {
  asm volatile("s_mov_b32 m0, %2\n\ts_nop 0\n\tglobal_load_lds_dwordx4 %0, %1" YSC " ; RING %3" ::"v"(voff), "s"(sbase), "s"(lds_dst), "i"(S) : "memory");
}
#define SBAR() do { asm volatile("" ::: "memory"); __builtin_amdgcn_s_barrier(); asm volatile("" ::: "memory"); } while (0)
#define K2ST(k, i)
#define K2OUT(k, w)
#define K2DECL
__device__ __forceinline__ void k2_load(const K2Args& A, unsigned lds0, int w, int ml, int mi, int nj) {
  const int lane = threadIdx.x & 63;
  K2DECL;
  K2ST(3, 0);
  char* ws = A.ws;
  const int r8 = lane >> 3, pc = lane & 7;
  const int rotg = (7 * ml) >> 1;
  const unsigned va = w * 2048 + lane * 16;
  const unsigned vw = (16 * w + r8) * 1024 + 16 * (pc ^ (((r8 >> 1) & 1) << 2));
  const unsigned vc = (8 * w + r8) * 1024 + 16 * (pc ^ ((4 * (w & 1) + (r8 >> 1)) & 7));
  auto issue = [&](auto sc) AI {
    constexpr int s = decltype(sc)::value;
    const unsigned sl = lds0 + (s & 3) * 32768;
    if constexpr (s >= 22) {
    } else if constexpr (s < 14) {
      const int gs = s + rotg >= 14 ? s + rotg - 14 : s + rotg;
      const char* ab = ws + OFF_A + (size_t)(mi * 14 + gs) * 16384;
      const char* wb = (const char*)A.wd + (size_t)gs * 131072 + nj * 128;
      dmar<s>(ab, va, sl + w * 2048);
      dmar<s>(ab + 1024, va, sl + w * 2048 + 1024);
      dmar<s>(wb, vw, sl + 16384 + w * 2048);
      dmar<s>(wb + 8192, vw, sl + 16384 + w * 2048 + 1024);
    } else {
      const int ys = (s - 14 + nj) & 7;
      const char* hb = ws + OFF_HF + (size_t)(mi * 8 + ys) * 16384;
      dmac<s>(hb, va, sl + w * 2048);
      dmac<s>(hb + 1024, va, sl + w * 2048 + 1024);
      dmar<s>((const char*)A.Cre + nj * 65536 + ys * 128, vc, sl + 16384 + w * 1024);
      dmar<s>((const char*)A.Cim + nj * 65536 + ys * 128, vc, sl + 24576 + w * 1024);
    }
  };
  {
    const int s0 = w >> 2;
    const int gs = s0 + rotg;
    const unsigned va0 = lane * 16, vw0 = r8 * 1024 + 16 * (pc ^ (((r8 >> 1) & 1) << 2));
    const char* ab = ws + OFF_A + (size_t)(mi * 14 + gs) * 16384 + (w & 3) * 4096;
    const char* wb = (const char*)A.wd + (size_t)gs * 131072 + nj * 128 + (w & 3) * 32768;
    const unsigned sl = lds0 + s0 * 32768 + (w & 3) * 4096;
#pragma unroll
    for (int d = 0; d < 4; ++d) {
      dma(ab + d * 1024, va0, sl + d * 1024);
      dma(wb + d * 8192, vw0, sl + 16384 + d * 1024);
    }
  }
  K2ST(3, 2);
  issue(IC<2>{});
  K2ST(3, 3);
  if (w < 4) VMCNT(4);
  SBAR();
  K2ST(3, 4);
  sfor<0, 21>([&](auto tc) AI {
    constexpr int t = decltype(tc)::value;
    if constexpr (t == XT) {
      dma((const char*)A.x + (size_t)mi * 65536 + nj * 128, (8 * w + r8) * 1024 + pc * 16, lds0 + L2_X + w * 1024);
      if (w == 0) dma(ws + OFF_ST + mi * 512, (lane & 31) * 16, lds0 + L2_X + 8192);
      if (w == 1) dma((const char*)A.Dk + nj * 128, pc * 16, lds0 + L2_X + 9216);
    }
    issue(IC<t + 3>{});
    RBAR(t + 1, (t + 2 < 22 ? 4 : 0) + (t + 3 < 22 ? 4 : 0));
    if (t & 1) K2ST(3, 5 + (t >> 1));
    if (t == 20) K2ST(3, 15);
  });
  K2OUT(3, w);
}

__device__ __forceinline__ float mulc(f32x2 c, float v) { return fmaf(c[0], v, SWP(v) * c[1]); }
__device__ __forceinline__ float fmac(f32x2 c, float v, float t) { return fmaf(SWP(v), c[1], fmaf(c[0], v, t)); }
__device__ __forceinline__ float bfadd(unsigned pair, unsigned selv, float c) {
  return __builtin_amdgcn_fdot2_f32_bf16(__builtin_bit_cast(bf16x2_t, pair), __builtin_bit_cast(bf16x2_t, selv), c, false);
}

struct K2Out {
  float sgv[8], xfv[8], rs;
  u32x2 xq[2], dq;
  f32x2 st[2];
  __device__ __forceinline__ void load(unsigned lds0, int w, int eh, int lane) {
    const int r = 32 * (w & 1) + 16 * eh + (lane >> 3);
    const unsigned co = (w >> 1) * 64 + (lane & 7) * 8;
#pragma unroll
    for (int e = 0; e < 2; ++e) {
      xq[e] = *LP(u32x2, lds0 + L2_X + (r + 8 * e) * 128 + co);
      st[e] = *LP(f32x2, lds0 + L2_X + 8192 + (r + 8 * e) * 8);
    }
    dq = *LP(u32x2, lds0 + L2_X + 9216 + co);
  }
  __device__ __forceinline__ void prep(int v) {
    const int e = v >> 2;
    if (!(v & 3)) rs = __builtin_amdgcn_rcpf(st[e][1]);
    const unsigned xw = xq[e][(v >> 1) & 1], dw = dq[(v >> 1) & 1];
    const float xx = bf2f((xw >> (16 * (v & 1))) & 0xffff), dd = bf2f((dw >> (16 * (v & 1))) & 0xffff);
    const float xn = rbf(rbf(xx - st[e][0]) * rs);
    xfv[v] = xx;
    sgv[v] = rbf(xn * dd);
    asm volatile("" : "+v"(xfv[v]), "+v"(sgv[v]));
  }
  __device__ __forceinline__ void finish(float* out, unsigned lds0, int w, int eh, int lane, int mi, int nj) {
    const int r8 = lane >> 3, pc = lane & 7;
    const unsigned exr = lds0 + w * 4096 + eh * 2048 + r8 * 128 + ((pc ^ r8) * 16);
    const unsigned eo = (64 * mi + 32 * (w & 1) + 16 * eh + r8) * D_ + 64 * nj + 32 * (w >> 1) + 4 * pc;
#pragma unroll
    for (int e = 0; e < 2; ++e) {
      const f32x4 y = *LP(f32x4, exr + L2_EXY + e * 1024), sv = *LP(f32x4, exr + L2_EXS + e * 1024);
      f32x4 o;
#pragma unroll
      for (int rr = 0; rr < 4; ++rr) o[rr] = fmaf(fmaf(2.f, y[rr], sgv[4 * e + rr]), sv[rr], xfv[4 * e + rr]);
      asm volatile("global_store_dwordx4 %0, %1, %2 sc0 sc1\n\ts_nop 1" ::"v"((eo + 8 * e * D_) * 4), "v"(o), "s"(out) : "memory");
    }
  }
};

__device__ __forceinline__ void k2_gate(const K2Args& A, unsigned lds0, int w, int mi, int nj) {
  const int lane = threadIdx.x & 63;
  char* ws = A.ws;
  K2DECL;
  K2ST(2, 0);
  RT0();
  const int wr = w & 1, wc = w >> 1;
  const int n = lane & 31, h = lane >> 5, t16 = lane & 15, g1 = (lane >> 4) & 1;
  const unsigned exw = lds0 + w * 4096 + n * 128 + ((h ^ (n & 7)) * 16);
  const unsigned arb = lds0 + wr * 1024 + lane * 16;
  const unsigned trw = lds0 + 16384 + (4 * h + (t16 >> 2)) * 128 + 32 * ((2 * wc + g1) ^ ((t16 >> 3) << 1)) + 8 * (t16 & 3);
  f32x16 acc;
#pragma unroll
  for (int i = 0; i < 16; ++i) acc[i] = 0.f;
  bf16x8 fa[4], fb[4];
  K2Out O;
  unsigned ga[2] = {arb, arb + 65536}, gb[2] = {trw, trw + 65536};
  asm volatile("" : "+v"(ga[1]), "+v"(gb[1]));
  auto rd = [&](auto uc) AI {
    constexpr int u = decltype(uc)::value, s = u >> 3, j = u & 7, so = (s & 1) * 32768, hf = (s >> 1) & 1;
    if constexpr (u < 112) {
      fa[u & 3] = *LP(bf16x8, ga[hf] + so + j * 2048);
      fb[u & 3] = trfrag(gb[hf] + so + j * 2048, 1024);
    }
  };
  SBAR();
  K2ST(2, 1);
  rd(IC<0>{}); rd(IC<1>{}); rd(IC<2>{});
  SCHED();
  sfor<0, 112>([&](auto uc) AI {
    constexpr int u = decltype(uc)::value;
    acc = MFMA32(fb[u & 3], fa[u & 3], acc);
    SCHED();
    if constexpr ((u & 7) == 5) BARRIER();
    rd(IC<u + 3>{});
    if constexpr (u == 74) O.load(lds0, w, 0, lane);
    if constexpr (u >= 78 && u < 110 && (u & 3) == 2) O.prep((u - 78) >> 2);
    SCHED();
    if ((u & 15) == 15) K2ST(2, 2 + (u >> 4));
  });
#pragma unroll
  for (int q = 0; q < 6; ++q) {
#pragma unroll
    for (int v = (16 * q) / 6; v < (16 * q + 16) / 6; ++v) {
      const float g = rbf(acc[v]);
      acc[v] = rbf(g * __builtin_amdgcn_rcpf(1.f + __expf(-g)));
    }
    asm volatile("" : "+v"(acc));
    SBAR();
  }
  SBAR();
#pragma unroll
  for (int g = 0; g < 4; ++g) {
    const f32x4 o = {acc[4 * g], acc[4 * g + 1], acc[4 * g + 2], acc[4 * g + 3]};
    const unsigned a = exw ^ (g << 5);
    asm volatile("ds_write_b128 %0, %1" ::"v"(a + L2_EXS), "v"(o) : "memory");
  }
  K2ST(2, 9);
  BARRIER();
  K2ST(2, 10);
  O.finish(A.out, lds0, w, 0, lane, mi, nj);
  K2ST(2, 11);
  RT1(1, blockIdx.x * 8 + w, ws);
  RT1(1, blockIdx.x * 8 + w + 4, ws);
}

__device__ __forceinline__ void k2_state(const K2Args& A, unsigned lds0, int w, int mi, int nj) {
  const int lane = threadIdx.x & 63, pi = mi >> 1;
  char* ws = A.ws;
  const int sg = lane >> 4;
  const unsigned cp = nj * 128 + 32 * w + (lane & 15);
  const unsigned hoff = w * 4096 + sg * 512 + (((lane & 15) ^ (sg & 1) ^ ((sg >> 1) << 3)) * 16);
  f32x2 a1[2], cr[2], fc[2];
  f32x4 tq[2], a4[2];
  float th[2];
  u32x4 hq[2][2];
  {
    const unsigned o0 = cp * 16, o1 = cp * 64 + (12 - 4 * sg) * 4, o2 = cp * 16 + (sg >> 1) * 16384 + (sg & 1) * 8;
    const char* b0 = ws + OFF_AT;
    const char* b0b = b0 + 256;
    GLD16S(a4[0], o0, b0); GLD16S(a4[1], o0, b0b);
#pragma unroll
    for (int j = 0; j < 2; ++j) {
      const char* b1 = ws + OFF_TOT + (pi - 16) * 4 + j * 1024;
      const char* b2 = ws + OFF_CAR + pi * 65536 + (mi & 1) * 32768 + j * 256;
      const char* b4 = ws + OFF_T0 + pi * 4096 + j * 64;
      GLD16S(tq[j], o1, b1); GLD8S(cr[j], o2, b2);
      asm volatile("global_load_dword %0, %1, %2" : "=v"(th[j]) : "v"(o0 >> 2), "s"(b4) : "memory");
    }
  }
  auto hload = [&](int j, int c) AI {
    const char* b3 = ws + OFF_H + (size_t)(mi * 8 + nj) * 16384 + j * 256 + c * 2048;
    GLD16S(hq[j][c], hoff, b3);
  };
  asm volatile("s_waitcnt vmcnt(6)" : "+v"(a4[0]), "+v"(a4[1])::"memory");
#pragma unroll
  for (int j = 0; j < 2; ++j) { a1[j] = (f32x2){a4[j][0], a4[j][1]}; fc[j] = (f32x2){a4[j][2], a4[j][3]}; }
  f32x2 a8[2], a16[2], a32[2], a64[2], aP[2], fF[2], fG[2];
#pragma unroll
  for (int j = 0; j < 2; ++j) {
    a8[j] = csq(csq(csq(a1[j]))); a16[j] = csq(a8[j]); a32[j] = csq(a16[j]); a64[j] = csq(a32[j]); aP[j] = csq(a64[j]);
  }
  asm volatile("" : "+v"(aP[0]), "+v"(aP[1]));
  SBAR();
  hload(0, 0); hload(0, 1);
  const f32x2 one = {1.f, 0.f};
#pragma unroll
  for (int j = 0; j < 2; ++j) {
    const f32x2 p4 = csq(csq(aP[j])), p8 = csq(p4);
    fF[j] = cmul(LB4 ? p4 : one, LLO ? one : p8);
    fG[j] = cmul(a1[j], cmul(LB4 ? a8[j] : one, LLO ? one : a16[j]));
  }
  asm volatile("" : "+v"(fF[0]), "+v"(fF[1]), "+v"(fG[0]), "+v"(fG[1]));
  SBAR();
  hload(1, 0); hload(1, 1);
  asm volatile("s_waitcnt vmcnt(4)" : "+v"(cr[0]), "+v"(cr[1]), "+v"(tq[0]), "+v"(tq[1]), "+v"(th[0]), "+v"(th[1])::"memory");
  unsigned slo = 0x00003F80u, shi = 0x3F800000u;
  asm volatile("" : "+s"(slo), "+s"(shi));
  float pr[2][2];
  {
    const int i0 = pi - 4 - 4 * sg;
    float hs[2], b[2];
#pragma unroll
    for (int j = 0; j < 2; ++j) {
      hs[j] = i0 >= 0 ? tq[j][0] : 0.f;
#pragma unroll
      for (int m = 1; m < 4; ++m) hs[j] = fmac(aP[j], hs[j], i0 + m >= 0 ? tq[j][m] : 0.f);
      hs[j] = mulc(fF[j], hs[j]);
      b[j] = hs[j];
    }
    asm volatile("s_nop 1\n\tv_permlane16_swap_b32 %0, %1\n\tv_permlane16_swap_b32 %2, %3" : "+v"(hs[0]), "+v"(b[0]), "+v"(hs[1]), "+v"(b[1]));
    hs[0] += b[0]; hs[1] += b[1];
    b[0] = hs[0]; b[1] = hs[1];
    asm volatile("s_nop 1\n\tv_permlane32_swap_b32 %0, %1\n\tv_permlane32_swap_b32 %2, %3" : "+v"(hs[0]), "+v"(b[0]), "+v"(hs[1]), "+v"(b[1]));
#pragma unroll
    for (int j = 0; j < 2; ++j) {
      const float se = hs[j] + b[j], g0 = mulc(fG[j], (mi & 1) ? fmac(a64[j], se, th[j]) : se), g1v = mulc(a32[j], g0);
      pr[j][0] = fmac(a1[j], cr[j][0], g0);
      pr[j][1] = fmac(a1[j], cr[j][1], g1v);
    }
    asm volatile("" : "+v"(pr[0][0]), "+v"(pr[0][1]), "+v"(pr[1][0]), "+v"(pr[1][1]));
  }
  SBAR();
#pragma unroll
  for (int j = 0; j < 2; ++j) {
    asm volatile("s_waitcnt vmcnt(2)" : "+v"(hq[j][0]), "+v"(hq[j][1])::"memory");
#pragma unroll
    for (int q = 0; q < 4; ++q) {
#pragma unroll
      for (int c = 0; c < 2; ++c) {
        const float p1 = mulc(a1[j], pr[j][c]);
        hq[j][c][q] = pk2(mulc(fc[j], bfadd(hq[j][c][q], slo, pr[j][c])), mulc(fc[j], bfadd(hq[j][c][q], shi, p1)));
        if (q < 3) pr[j][c] = mulc(a1[j], p1);
      }
    }
#pragma unroll
    for (int c = 0; c < 2; ++c) {
      const char* sb = ws + OFF_HF + (size_t)(mi * 8 + nj) * 16384 + j * 256 + c * 2048;
      GST16S(sb, hoff, hq[j][c]);
    }
    SBAR();
  }
  SBAR();
  SBAR();
  VMCNT(0);
  SBAR();
  const char* cb = ws + OFF_CNT + mi * 128;
  const unsigned vz = 0;
  unsigned cnt;
  if (w == 0 && lane == 0) asm volatile("global_atomic_add %0, %1, %2 sc1" ::"v"(vz), "v"(1u), "s"(cb) : "memory");
  SBAR();
  SBAR();
  if (w == 0) asm volatile("global_load_dword %0, %1, %2 sc1" : "=v"(cnt) : "v"(vz), "s"(cb) : "memory");
  const int wr = w & 1, wc = w >> 1;
  const int n = lane & 31, h = lane >> 5, t16 = lane & 15, g1 = (lane >> 4) & 1;
  const unsigned exw = lds0 + w * 4096 + n * 128 + ((h ^ (n & 7)) * 16);
  const unsigned trh = lds0 + (4 * wr + 2 * g1 + ((t16 >> 1) & 1)) * 512 + ((16 * h + 8 * g1 + 2 * (t16 >> 2) + ((t16 >> 1) & 1)) * 16) + (t16 & 1) * 8;
  const int cd = 32 * wc + n;
  const unsigned crb = lds0 + 16384 + cd * 128 + 16 * (h ^ ((cd >> 1) & 7));
  unsigned ya[2][4], yc[2][4];
#pragma unroll
  for (int i = 0; i < 4; ++i) {
    ya[0][i] = trh ^ ((i & 1) * 16) ^ ((i >> 1) * 128); ya[1][i] = ya[0][i] + 65536;
    yc[0][i] = crb ^ (i << 5); yc[1][i] = yc[0][i] + 65536;
  }
  asm volatile("" : "+v"(ya[1][0]), "+v"(ya[1][1]), "+v"(ya[1][2]), "+v"(ya[1][3]));
  asm volatile("" : "+v"(yc[1][0]), "+v"(yc[1][1]), "+v"(yc[1][2]), "+v"(yc[1][3]));
  SBAR();
  if (w == 0) {
    asm volatile("s_waitcnt vmcnt(0)" : "+v"(cnt)::"memory");
    while (__builtin_amdgcn_readfirstlane(cnt) < 8u) {
      __builtin_amdgcn_s_sleep(2);
      asm volatile("global_load_dword %0, %1, %2 sc1\n\ts_waitcnt vmcnt(0)" : "=v"(cnt) : "v"(vz), "s"(cb) : "memory");
    }
  }
  SBAR();
  K2Out O;
  O.load(lds0, w, 1, lane);
#pragma unroll
  for (int v = 0; v < 3; ++v) O.prep(v);
  SBAR();
#pragma unroll
  for (int v = 3; v < 6; ++v) O.prep(v);
  SBAR();
#pragma unroll
  for (int v = 6; v < 8; ++v) O.prep(v);
  f32x16 acc;
#pragma unroll
  for (int i = 0; i < 16; ++i) acc[i] = 0.f;
  bf16x8 fa[4], fb[4];
  auto rd = [&](auto uc) AI {
    constexpr int u = decltype(uc)::value, s = 14 + (u >> 3), i = (u >> 1) & 3, part = u & 1, so = (s & 1) * 32768, hf = (s >> 1) & 1;
    if constexpr (u < 64) {
      fa[u & 3] = trfrag2(ya[hf][part] + so + i * 4096, ya[hf][2 + part] + so + i * 4096);
      fb[u & 3] = *LP(bf16x8, yc[hf][i] + so + part * 8192);
    }
  };
  __builtin_amdgcn_s_setprio(2);
  SBAR();
  rd(IC<0>{}); rd(IC<1>{}); rd(IC<2>{});
  SCHED();
  sfor<0, 64>([&](auto uc) AI {
    constexpr int u = decltype(uc)::value;
    acc = MFMA32(fb[u & 3], fa[u & 3], acc);
    SCHED();
    if constexpr ((u & 7) == 5 && u < 56) BARRIER();
    rd(IC<u + 3>{});
    SCHED();
  });
#pragma unroll
  for (int g = 0; g < 4; ++g)
    *LP(f32x4, (exw ^ (g << 5)) + L2_EXY) = (f32x4){acc[4 * g], acc[4 * g + 1], acc[4 * g + 2], acc[4 * g + 3]};
  BARRIER();
  O.finish(A.out, lds0, w, 1, lane, mi, nj);
}

__global__ __launch_bounds__(1024, 4) NOPK void k2(K2Args A) {
  extern __shared__ __attribute__((aligned(16))) char smem[];
  const unsigned lds0 = (unsigned)(uintptr_t)(LAS char*)smem;
  const int w = __builtin_amdgcn_readfirstlane(threadIdx.x >> 6) & 15;
  const int b = blockIdx.x;
  const int xcd = b & 7, r = b >> 3;
  const int ml = r >> 3, mi = xcd * 4 + ml, nj = r & 7;
  if (w < 4) {
    k2_state(A, lds0, w, mi, nj);
  } else if (w < 12) {
    k2_load(A, lds0, w - 4, ml, mi, nj);
  } else {
    __builtin_amdgcn_s_setprio(1);
    k2_gate(A, lds0, w & 3, mi, nj);
  }
  asm volatile("k2_code_end:");
}

extern "C" void kernel_launch(void* const* d_in, const int* in_sizes, int n_in, void* d_out, int out_size, void* d_ws,
                              size_t ws_size, hipStream_t stream) {
  K1Args a;
  a.x = (const u16*)d_in[0]; a.Lre = (const u16*)d_in[1]; a.Lim = (const u16*)d_in[2];
  a.Bre = (const u16*)d_in[3]; a.Bim = (const u16*)d_in[4]; a.Cre = (const u16*)d_in[5]; a.Cim = (const u16*)d_in[6];
  a.ldt = (const u16*)d_in[7]; a.wg = (const u16*)d_in[10]; a.wu = (const u16*)d_in[11]; a.wd = (const u16*)d_in[12];
  a.ws = (char*)d_ws;
  (void)hipFuncSetAttribute((const void*)k1, hipFuncAttributeMaxDynamicSharedMemorySize, L1_SIZE);
  (void)hipFuncSetAttribute((const void*)k2, hipFuncAttributeMaxDynamicSharedMemorySize, L2_SIZE);
  k1<<<256, 512, L1_SIZE, stream>>>(a);
  K2Args b;
  b.x = (const u16*)d_in[0]; b.Dk = (const u16*)d_in[8]; b.wd = (const u16*)d_in[12];
  b.Cre = (const u16*)d_in[5]; b.Cim = (const u16*)d_in[6];
  b.ws = (char*)d_ws; b.out = (float*)d_out;
  k2<<<256, 1024, L2_SIZE, stream>>>(b);
}
```

```cpp
#include <hip/hip_runtime.h>
#include <hip/hip_bf16.h>
#include <stdint.h>

typedef unsigned short u16;
typedef __attribute__((ext_vector_type(8))) short bf16x8;
typedef __attribute__((ext_vector_type(4))) short s16x4;
typedef __attribute__((ext_vector_type(16))) float f32x16;
typedef __attribute__((ext_vector_type(4))) float f32x4;
typedef __attribute__((ext_vector_type(2))) float f32x2;
typedef __attribute__((ext_vector_type(4))) unsigned u32x4;
typedef __attribute__((ext_vector_type(2))) unsigned u32x2;
typedef __attribute__((ext_vector_type(2))) __bf16 bf16x2_t;
#define LAS __attribute__((address_space(3)))
#if defined(__HIP_DEVICE_COMPILE__)
#define NOPK __attribute__((target("no-packed-fp32-ops")))
#else
#define NOPK
#endif

constexpr int D_ = 512;

constexpr size_t OFF_A   = 0;
constexpr size_t OFF_H   = OFF_A + (size_t)32 * 112 * 2048;
constexpr size_t OFF_CAR = OFF_H + (size_t)32 * 32 * 4096;
constexpr size_t OFF_TOT = OFF_CAR + (size_t)16 * 1024 * 64;
constexpr size_t OFF_AT  = OFF_TOT + (size_t)16 * 4096;
constexpr size_t OFF_ST  = OFF_AT + 16384;
constexpr size_t OFF_CNT = OFF_ST + 16384;
constexpr size_t OFF_T0  = OFF_CNT + 4096;
constexpr size_t OFF_HF  = OFF_T0 + 65536;

constexpr int L1_AGG = 131072;
constexpr int L1_AC  = L1_AGG + 18432;
constexpr int L1_STF = L1_AC + 4608;
constexpr int L1_ST  = L1_STF + 1024;
constexpr int L1_CS  = L1_ST + 2048;
constexpr int L1_DUM = L1_CS + 5120;
constexpr int L1_SIZE = L1_DUM + 1024;

__device__ __forceinline__ float bf2f(unsigned h) { return __builtin_bit_cast(float, h << 16); }
__device__ __forceinline__ float rbf(float v) { return (float)(__bf16)v; }
__device__ __forceinline__ unsigned pk2(float lo, float hi) {
  bf16x2_t r = {(__bf16)lo, (__bf16)hi};
  return __builtin_bit_cast(unsigned, r);
}
template <int CTRL> __device__ __forceinline__ float dpp0(float x) {
  return __builtin_bit_cast(float, __builtin_amdgcn_update_dpp(0, __builtin_bit_cast(int, x), CTRL, 0xf, 0xf, true));
}
#define SWP(x) dpp0<0xB1>(x)
__device__ __forceinline__ f32x2 csq(f32x2 a) { return (f32x2){fmaf(a[0], a[0], -a[1] * a[1]), 2.f * a[0] * a[1]}; }
__device__ __forceinline__ f32x2 cmul(f32x2 a, f32x2 b) {
  return (f32x2){fmaf(a[0], b[0], -a[1] * b[1]), fmaf(a[0], b[1], a[1] * b[0])};
}
__device__ __forceinline__ f32x2 cfma(f32x2 a, f32x2 b, f32x2 c) {
  return (f32x2){fmaf(a[0], b[0], fmaf(-a[1], b[1], c[0])), fmaf(a[0], b[1], fmaf(a[1], b[0], c[1]))};
}
template <int V> struct IC { static constexpr int value = V; };
template <int I, int N, class F> __device__ __forceinline__ void sfor(F&& f) {
  if constexpr (I < N) { f(IC<I>{}); sfor<I + 1, N>(f); }
}
__device__ __forceinline__ const char* uptr(const void* base, unsigned off) {
  return (const char*)base + (size_t)(unsigned)__builtin_amdgcn_readfirstlane(off);
}
#define MFMA16(a, b, c) __builtin_amdgcn_mfma_f32_16x16x32_bf16(a, b, c, 0, 0, 0)
#define MFMA32(a, b, c) __builtin_amdgcn_mfma_f32_32x32x16_bf16(a, b, c, 0, 0, 0)
#define SCHED() __builtin_amdgcn_sched_barrier(0)
#define LP(T, a) ((LAS T*)(unsigned)(a))
#define AI __attribute__((always_inline))
#define LMASK(m) __builtin_amdgcn_inverse_ballot_w64(m)
#define LB0 LMASK(0xAAAAAAAAAAAAAAAAull)
#define LB1 LMASK(0xCCCCCCCCCCCCCCCCull)
#define LB3 LMASK(0xFF00FF00FF00FF00ull)
#define LB4 LMASK(0xFFFF0000FFFF0000ull)
#define LLO LMASK(0x00000000FFFFFFFFull)

#define GLD16(dst, p) asm volatile("global_load_dwordx4 %0, %1, off" : "=v"(dst) : "v"(p) : "memory")
#define GLD8(dst, p) asm volatile("global_load_dwordx2 %0, %1, off" : "=v"(dst) : "v"(p) : "memory")
#define GLD2(dst, p) asm volatile("global_load_ushort %0, %1, off" : "=v"(dst) : "v"(p) : "memory")
#define GLD16S(dst, vo, sb) asm volatile("global_load_dwordx4 %0, %1, %2" : "=v"(dst) : "v"(vo), "s"(sb) : "memory")
#define GLD8S(dst, vo, sb) asm volatile("global_load_dwordx2 %0, %1, %2" : "=v"(dst) : "v"(vo), "s"(sb) : "memory")
#define GST16S(sb, vo, v) asm volatile("global_store_dwordx4 %0, %1, %2 sc0 sc1\n\ts_nop 1" ::"v"(vo), "v"(v), "s"(sb) : "memory")
#define LDSW(a, off, v) asm volatile("ds_write_b128 %0, %1 offset:%2" ::"v"(a), "v"(v), "i"(off) : "memory")
#define VMCNT(n) asm volatile("s_waitcnt vmcnt(%0)" ::"i"(n) : "memory")
#define BARRIER() do { asm volatile("s_waitcnt lgkmcnt(0)" ::: "memory"); __builtin_amdgcn_s_barrier(); asm volatile("" ::: "memory"); } while (0)
#define RBAR(s, n) do { asm volatile("s_waitcnt vmcnt(%0) lgkmcnt(0) ; NEED %1" ::"i"(n), "i"(s) : "memory"); __builtin_amdgcn_s_barrier(); asm volatile("" ::: "memory"); } while (0)
__device__ __forceinline__ void dma(const char* sbase, unsigned voff, unsigned lds_dst) {
  asm volatile("s_mov_b32 m0, %2\n\ts_nop 0\n\tglobal_load_lds_dwordx4 %0, %1" ::"v"(voff), "s"(sbase), "s"(lds_dst) : "memory");
}
template <int S> __device__ __forceinline__ void dmar(const char* sbase, unsigned voff, unsigned lds_dst) {
  asm volatile("s_mov_b32 m0, %2\n\ts_nop 0\n\tglobal_load_lds_dwordx4 %0, %1 ; RING %3" ::"v"(voff), "s"(sbase), "s"(lds_dst), "i"(S) : "memory");
}
__device__ __forceinline__ bf16x8 trfrag2(unsigned a, unsigned b) {
  s16x4 lo = __builtin_amdgcn_ds_read_tr16_b64_v4i16(LP(s16x4, a));
  s16x4 hi = __builtin_amdgcn_ds_read_tr16_b64_v4i16(LP(s16x4, b));
  return __builtin_shufflevector(lo, hi, 0, 1, 2, 3, 4, 5, 6, 7);
}
__device__ __forceinline__ bf16x8 trfrag(unsigned a, int d) {
  s16x4 lo = __builtin_amdgcn_ds_read_tr16_b64_v4i16(LP(s16x4, a));
  s16x4 hi = __builtin_amdgcn_ds_read_tr16_b64_v4i16(LP(s16x4, a + d));
  return __builtin_shufflevector(lo, hi, 0, 1, 2, 3, 4, 5, 6, 7);
}

#define STAMP(i)
constexpr size_t OFF_DBG = (size_t)64 << 20;
#define RT0()
#define RT1(a,b,c)

struct K1Args {
  const u16 *x, *Lre, *Lim, *Bre, *Bim, *Cre, *Cim, *ldt, *wg, *wu, *wd;
  char* ws;
};

constexpr int k1_bar_unit(int s) { return 16 * (s - 8) - 3; }
constexpr int k1_ival(int u) { return 8 + (u + 3) / 16; }
#define DM12 25
constexpr int k1_dma_unit(int s, int d) {
  return s == 10 ? 2 + 3 * d : (s == 11 ? 14 + 3 * d : (s == 12 ? (d < 2 ? DM12 + 2 * d : 26 + 2 * d) : (s == 13 ? 35 + 3 * d : k1_bar_unit(s - 3) + 1 + 4 * d)));
}
#define PT0 33
#define PF0 76
#define PFD 3
#define NPI 9
constexpr int K1_NPI = NPI;
constexpr int k1_pf_unit(int i) { return PF0 + PFD * i; }
constexpr int k1_s_unit(int p, int i) { return 33 + 32 * p + (3 * i) / 2; }
constexpr int k1_extra(bool gt, int u) {
  int n = u == 28;
  for (int i = 0; i < K1_NPI; ++i) n += gt && k1_pf_unit(i) == u;
  n += gt && u >= PT0 && u < PT0 + 3;
  if (gt) n += (u == 69) + (u == 73);
  else
    for (int p = 0; p < 3; ++p) n += (k1_s_unit(p, 14) == u) + (k1_s_unit(p, 15) == u);
  return n;
}
constexpr int k1_wait(bool gt, int s) {
  if (s < 9) return s == 0 ? 10 : (s < 3 ? 7 : (s < 6 ? 6 : (s == 6 ? 5 : (s == 7 ? 4 : 2))));
  const int u0 = s >= 10 ? k1_dma_unit(s, 3) : 0;
  int n = 0;
  for (int s2 = 10; s2 < 16; ++s2)
    for (int d = 0; d < 4; ++d) n += s2 > s && k1_dma_unit(s2, d) < k1_bar_unit(s);
  for (int u = u0; u < k1_bar_unit(s); ++u) n += k1_extra(gt, u);
  return n;
}

#define PFW 7
template <bool GT>
__device__ __forceinline__ void k1_body(const K1Args& A, char* smem, int pi, int nb, int c0, int rx, unsigned long long cpc,
                                        unsigned clen) {
  const int tid = threadIdx.x, lane = tid & 63;
  const int w = __builtin_amdgcn_readfirstlane(tid >> 6) & 7;
  const int rg = w & 3, cg = w >> 2, X = rg ^ cg;
  STAMP(0);
  RT0();
  const unsigned lds0 = (unsigned)(uintptr_t)(LAS char*)smem;
  const int r8 = lane >> 3, pc = lane & 7;
  const int tb0 = GT ? nb - 8 : 14 + 9 * (nb - 14);
  auto olane = [&]() AI -> int { int l = lane; asm volatile("" : "+v"(l)); return l; };

  const char* xb = uptr(A.x, pi * 131072 + w * 16384);
  const unsigned vs = r8 * 1024 + 16 * (pc ^ ((r8 >> 1) | ((w & 1) << 2)));
  const unsigned vw = GT ? (8 * w + r8) * 3584 + 16 * (pc ^ (((r8 >> 1) & 1) << 2)) : vs;
  const char* bmat = uptr((w & 1) ? A.Bim : A.Bre, ((GT ? nb : tb0) * 16 + 8 * ((w >> 1) & 1)) * 1024);
  auto wbase = [&](int p, int c) AI -> const char* {
    if (GT) return (const char*)((p & 1) ? A.wu : A.wg) + (size_t)c * (64 * 3584) + (128 * nb + 64 * (p >> 1)) * 2;
    return bmat + (unsigned)((4 * cg + p) * 16384 + c * 128);
  };
  auto issue = [&](auto sc, int d) AI {
    constexpr int s = decltype(sc)::value;
    const unsigned dst = lds0 + (s & 3) * 32768;
    if constexpr (s < 8) {
      const int c = (c0 + s) & 7;
      if (d == 0) dmar<s>(xb + c * 128, vs, dst + w * 2048);
      if (d == 1)
        asm volatile("s_mov_b32 m0, %2\n\ts_cmp_eq_u32 %3, %4\n\ts_cselect_b64 exec, -1, 0\n\tglobal_load_lds_dwordx4 %0, %1 ; RING %5\n\ts_mov_b64 exec, -1"
                     ::"v"(vs), "s"(uptr(bmat, (GT ? 0 : 8 * 16384) + c * 128)), "s"(dst + 16384 + rg * 1024), "s"(cg), "i"(s & 1), "i"(s) : "memory", "scc");
      if (d == 2) dmar<s>(xb + c * 128 + 8192, vs, dst + w * 2048 + 1024);
    } else if constexpr (s < 16) {
      dmar<s>(wbase((s - 8) >> 1, (c0 + 4 * (s & 1) + d) & 7), vw, dst + d * 8192 + w * 1024);
    }
  };
  const int gx = rx < 24 ? rx : 24 + (rx & 3);
  auto prefetch = [&](int i) AI {
    const char* sb = i < 8 ? (const char*)A.wd + (gx + 28 * i) * 8192
                           : (i < 12 ? (const char*)(i < 10 ? A.Cre : A.Cim) + (gx + 28 * (i & 1)) * 8192
                                     : (const char*)(gx < 8 ? A.Cre : A.Cim) + (56 + (gx & 7)) * 8192);
    const unsigned vo = olane() * 128;
    asm volatile("s_mov_b32 m0, %2\n\ts_cmp_eq_u32 %3, %4\n\ts_cselect_b64 exec, -1, 0\n\tglobal_load_lds_dword %0, %1\n\ts_mov_b64 exec, -1"
                 ::"v"(vo), "s"(sb), "s"(lds0 + L1_DUM), "s"(i == 12 ? w + (gx & 16) : w), "i"(i & 7) : "memory", "scc");
  };
  auto ptouch = [&](int i) AI {
    const char* sb = (const char*)(i == 0 ? A.wd : (i == 1 ? A.Cre : A.Cim)) + gx * 8192;
    const unsigned vo = olane() * (28 * 8192);
    asm volatile("s_mov_b32 m0, %2\n\ts_cmp_eq_u32 %3, %4\n\ts_cselect_b64 exec, %5, 0\n\tglobal_load_lds_dword %0, %1\n\ts_mov_b64 exec, -1"
                 ::"v"(vo), "s"(sb), "s"(lds0 + L1_DUM), "s"(w), "i"(5 + i), "i"(i == 0 ? 0xff : 3) : "memory", "scc");
  };
  issue(IC<0>{}, 0); issue(IC<0>{}, 1); issue(IC<0>{}, 2);
  auto warm = [&]() AI {
    unsigned off = (rx & 15) * 4096 + lane * 64;
    off = off < clen ? off : 0;
    asm volatile("s_mov_b32 m0, %2\n\ts_cmp_eq_u32 %3, 7\n\ts_cselect_b64 exec, -1, 0\n\tglobal_load_lds_dword %0, %1\n\ts_mov_b64 exec, -1"
                 ::"v"(off), "s"(cpc), "s"(lds0 + L1_DUM), "s"(w) : "memory", "scc");
  };
  unsigned plr, pli, pdt;
  const int tsp = GT ? 8 : ((tid >> 5) < 8 ? (tid >> 5) : 8);
  {
    const int ch = 16 * (tb0 + tsp) + ((tid & 31) >> 1);
    GLD2(plr, A.Lre + ch); GLD2(pli, A.Lim + ch); GLD2(pdt, A.ldt + ch);
  }
  __builtin_amdgcn_s_barrier();
  issue(IC<1>{}, 0); issue(IC<1>{}, 1); issue(IC<1>{}, 2);
  __builtin_amdgcn_s_barrier();
  issue(IC<2>{}, 0); issue(IC<2>{}, 1); issue(IC<2>{}, 2);
  warm();
  STAMP(1);
  char* ws = A.ws;
  float pe, pr_, pxx;
  f32x2 aco;
  int pq;
  auto param = [&](int k) AI {
    const float lr = bf2f(plr), li = bf2f(pli);
    if (k == 0) {
      asm volatile("" : "+v"(plr), "+v"(pli), "+v"(pdt));
      pxx = rbf(expf(bf2f(pdt)));
      asm volatile("" : "+v"(pxx));
    } else if (k == 1) {
      pe = expf(pxx * lr);
      const float th = pxx * li;
      const float kk = __builtin_rintf(th * 0.15915494f);
      const float r = fmaf(th, 6.4206383e-09f, fmaf(th, 0.15915494f, -kk));
      const float q = __builtin_rintf(4.f * r);
      pq = (int)q;
      pr_ = fmaf(-0.25f, q, r) * 6.2831855f;
      asm volatile("" : "+v"(pe), "+v"(pr_), "+v"(pq));
    } else if (k == 2) {
      const float z = pr_ * pr_;
      const float sn = fmaf(pr_ * z, fmaf(z, fmaf(z, -1.9515296e-4f, 8.3321609e-3f), -1.6666655e-1f), pr_);
      const float cs = fmaf(z * z, fmaf(z, fmaf(z, 2.4433157e-5f, -1.3887316e-3f), 4.1666646e-2f), fmaf(z, -0.5f, 1.f));
      const float c1 = (pq & 1) ? sn : cs, s1 = (pq & 1) ? cs : sn;
      aco = (f32x2){pe * (((pq + 1) & 2) ? -c1 : c1), pe * ((pq & 2) ? -s1 : s1)};
      asm volatile("" : "+v"(aco));
    } else if (k == 3) {
      const bool im = tid & 1;
      if (pi == 0) {
        const float ar = aco[0], ai = aco[1], nr = ar - 1.f, den = lr * lr + li * li;
        const float fr = (nr * lr + ai * li) / den, fi = (ai * lr - nr * li) / den;
        const f32x4 ac = {ar, im ? ai : -ai, im ? -fr : fr, -fi};
        if (tid < (GT ? 32 : 288)) *(f32x4*)(ws + OFF_AT + (size_t)((tb0 + tsp) * 32 + (tid & 31)) * 16) = ac;
      }
      aco[1] = im ? aco[1] : -aco[1];
      asm volatile("" : "+v"(aco));
      if (pi == 1 && nb == 0 && tid < 32) *(unsigned*)(ws + OFF_CNT + tid * 128) = 0u;
    }
  };

  const int n = lane & 31, h = lane >> 5, t16 = lane & 15;
  const int rho = 16 * ((n >> 2) & 1) + 4 * (n >> 3) + (n & 3);
  const int tau = rho ^ (8 * cg);
  const unsigned selw = (t16 == 4 * ((lane >> 4) & 1)) ? 0x3F803F80u : 0u;
  const bf16x8 sel = __builtin_bit_cast(bf16x8, (u32x4){selw, selw, selw, selw});
  const unsigned axb = lds0 + (32 * rg + tau) * 128 + 16 * ((2 * X + h) ^ (((tau >> 1) & 3) | ((tau >> 4) << 2)));
  auto sbase = [&](int l) AI -> unsigned {
    const int nn = l & 31, hh = l >> 5;
    return lds0 + (16 * (nn >> 4) + 8 * (nn & 1) + ((nn >> 1) & 7)) * 128 + 16 * ((2 * X + hh) ^ (((nn >> 2) & 3) | ((nn & 1) << 2)));
  };
  unsigned aw[4];
  {
    const unsigned b = GT ? lds0 + (16 * X + 8 * h + (t16 >> 2)) * 128 + 32 * ((2 * cg + ((lane >> 4) & 1)) ^ ((t16 >> 3) << 1)) + 8 * (t16 & 3)
                          : sbase(lane) + cg * 4096;
#pragma unroll
    for (int j = 0; j < 4; ++j) aw[j] = b ^ (GT ? j << 11 : j << 5);
  }
  auto flip = [&]() AI {
#pragma unroll
    for (int j = 0; j < 4; ++j) aw[j] ^= 65536;
    asm volatile("" : "+v"(aw[0]), "+v"(aw[1]), "+v"(aw[2]), "+v"(aw[3]));
  };
  auto wfrag = [&](unsigned a) AI -> bf16x8 {
    if (GT) return trfrag(a, 512);
    return *LP(bf16x8, a);
  };
  auto mm = [&](bf16x8 wfv, bf16x8 xfv, f32x16 c) AI -> f32x16 { return GT ? MFMA32(wfv, xfv, c) : MFMA32(xfv, wfv, c); };

  const f32x4 zero4 = {0.f, 0.f, 0.f, 0.f};
  f32x16 zero16;
#pragma unroll
  for (int i = 0; i < 16; ++i) zero16[i] = 0.f;
  f32x16 acc[4], accs = zero16, gr = zero16;
  f32x4 sm = zero4, csa = zero4;
  bf16x8 xf[32];
  bf16x8 wd2;
  const unsigned asb = sbase(lane) + 16384;
  sfor<0, 8>([&](auto ic) AI {
    constexpr int i = decltype(ic)::value;
    RBAR(i, k1_wait(GT, i));
    if (i == 0) STAMP(2);
    if (i == 4) STAMP(3);
    constexpr int so = (i & 1) * 32768;
    const unsigned ax = axb + (i & 2) * 32768, as = asb + (i & 2) * 32768;
    xf[4 * i] = *LP(bf16x8, ax + so);
    const bf16x8 w0 = *LP(bf16x8, as + so);
    xf[4 * i + 2] = *LP(bf16x8, (ax ^ 64) + so);
    const bf16x8 w2 = *LP(bf16x8, (as ^ 64) + so);
    xf[4 * i + 1] = *LP(bf16x8, (ax ^ 32) + so);
    xf[4 * i + 3] = *LP(bf16x8, (ax ^ 96) + so);
    SCHED();
    constexpr int sn = i < 5 ? i + 3 : (i < 7 ? 8 : 9), d0 = i == 6 ? 2 : 0;
    if (i > 0) { gr = MFMA32(xf[4 * i - 2], xf[4 * i - 2], gr); SCHED(); }
    issue(IC<sn>{}, d0);
    SCHED();
    if (i > 0) {
      accs = MFMA32(xf[4 * i - 2], wd2, accs);
      sm = MFMA16(sel, xf[4 * i - 2], sm);
      SCHED();
    }
    issue(IC<sn>{}, d0 + 1);
    SCHED();
    gr = MFMA32(xf[4 * i], xf[4 * i], gr);
    sm = MFMA16(sel, xf[4 * i], sm);
    SCHED();
    if (i < 5) issue(IC<sn>{}, 2);
    SCHED();
    accs = MFMA32(xf[4 * i], w0, accs);
    csa = MFMA16(sel, w0, csa);
    SCHED();
    if (i >= 1 && i < 5) param(i - 1);
    wd2 = w2;
    SCHED();
  });

  float sdv, rsg;
  float dgt[4];
  f32x2 stp;
  f32x4 rq[2];
  f32x2 acv;
  float zc[2], tg, csr;
  unsigned ob[8];
  f32x4 pv[2], stq;
  f32x2 cs2;
  u32x4 mB, cA;
  unsigned acs = lds0 + L1_CS + cg * 2560 + n * 16 + h * 8, arq = lds0 + L1_STF + w * 128 + h * 64;
  asm volatile("" : "+v"(acs), "+v"(arq));
  auto cs_put = [&](int p) AI {
    unsigned a = acs;
    asm volatile("" : "+v"(a));
    if (LLO) *LP(float, a + X * 4 + p * 512) = csa[0];
  };
  auto cs_get = [&](int p) AI { cs2 = *LP(f32x2, acs + p * 512); };
  auto cs_split = [&](int k) AI {
    if (k == 0) {
      const float c = cs2[0] + cs2[1];
      const unsigned c0 = __builtin_bit_cast(unsigned, c) & 0xffff0000u;
      csr = c - __builtin_bit_cast(float, c0);
      cA[0] = c0;
      cA[3] = 0u;
    } else {
      const unsigned c1 = __builtin_bit_cast(unsigned, csr) & 0xffff0000u;
      const float r2 = csr - __builtin_bit_cast(float, c1);
      cA[1] = c1;
      cA[2] = __builtin_bit_cast(unsigned, r2) & 0xffff0000u;
    }
    asm volatile("" : "+v"(cA));
  };
  auto corr = [&](f32x16& a, bool ssm) AI {
    const bf16x8 m = __builtin_bit_cast(bf16x8, mB), c = __builtin_bit_cast(bf16x8, cA);
    a = ssm ? MFMA32(m, c, a) : MFMA32(c, m, a);
  };
  auto lsel = [&](float a, float b, unsigned long long m) AI -> float {
    float d;
    asm volatile("v_cndmask_b32_e64 %0, %1, %2, %3" : "=v"(d) : "v"(a), "v"(b), "s"(m));
    return d;
  };
  auto stat_q = [&](int k) AI {
    const int l = olane();
    if (k < 4) {
      const float lo = lsel(gr[4 * k], gr[4 * k + 1], 0xAAAAAAAAAAAAAAAAull), hi = lsel(gr[4 * k + 2], gr[4 * k + 3], 0xAAAAAAAAAAAAAAAAull);
      dgt[k] = lsel(lo, hi, 0xCCCCCCCCCCCCCCCCull);
    } else if (k == 4) {
      const float lo = lsel(dgt[0], dgt[1], 0xFF00FF00FF00FF00ull), hi = lsel(dgt[2], dgt[3], 0xFF00FF00FF00FF00ull);
      dgt[0] = lsel(lo, hi, 0xFFFF0000FFFF0000ull);
    } else if (k == 5) {
      const unsigned a = lds0 + L1_ST + (32 * rg + tau) * 16 + cg * 8;
      if (LMASK(0xF0F0F0F00F0F0F0Full)) *LP(float, a) = dgt[0];
      if (LLO) *LP(float, a + 4) = sm[0];
    } else if (k == 6) {
      cs_put(4);
    } else if (k == 7) {
      if (!GT) *LP(f32x2, lds0 + L1_AC + (tsp * 32 + (l & 31)) * 8) = aco;
    } else if (k == 8) {
      stq = *LP(f32x4, lds0 + L1_ST + (32 * rg + tau) * 16);
    } else if (k == 9) {
      const float mean = (stq[1] + stq[3]) * (1.f / 512.f);
      const float var = fmaxf(fmaf(stq[0] + stq[2], 1.f / 512.f, -mean * mean), 0.f);
      stp = (f32x2){rbf(mean), var};
    } else if (k == 10) {
      sdv = rbf(__builtin_amdgcn_sqrtf(rbf(rbf(stp[1]) + 1e-5f)));
    } else if (k == 11) {
      const unsigned nm = __builtin_bit_cast(unsigned, -stp[0]);
      mB = (u32x4){nm, nm, nm, 0u};
      asm volatile("" : "+v"(mB));
    } else if (k == 12) {
      const float rs = __builtin_amdgcn_rcpf(sdv);
      rsg = rs * -1.44269504088896f;
      *LP(float, lds0 + L1_STF + w * 128 + rho * 4) = rs;
      if (nb == 0 && cg == 0 && l < 32) *(f32x2*)(ws + OFF_ST + (size_t)(pi * 128 + rg * 32 + tau) * 8) = (f32x2){stp[0], sdv};
    }
  };
  auto gate_q = [&](int p, int hq) AI {
    const int v = hq >> 1;
    if (!(hq & 1)) {
      tg = __builtin_amdgcn_exp2f(acc[p][v] * rsg);
    } else {
      float sv = __builtin_amdgcn_rcpf(fmaf(tg, sdv, sdv));
      asm volatile("" : "+v"(sv));
      acc[p][v] = sv;
    }
  };
  auto up_q = [&](int p, int k) AI {
    const int v = 2 * k;
    ob[k] = pk2(acc[p][v] * acc[p - 1][v], acc[p][v + 1] * acc[p - 1][v + 1]);
  };
  auto up_st = [&](int p, int e) AI {
    const u32x4 o = {ob[4 * e], ob[4 * e + 1], ob[4 * e + 2], ob[4 * e + 3]};
    const char* sb = uptr(ws, (unsigned)OFF_A + (((2 * pi + (rg >> 1)) * 112 + 8 * nb + 4 * (p >> 1) + 2 * cg + e) * 2 + (rg & 1)) * 1024);
    const unsigned vo = (tau + (olane() & 32)) * 16;
    GST16S(sb, vo, o);
  };
  auto ssm_pre = [&](int ts) AI { acv = GT ? aco : *LP(f32x2, lds0 + L1_AC + (ts * 32 + (olane() & 31)) * 8); };
  auto ssm_mr = [&](int c, int g) AI { rq[c] = *LP(f32x4, arq + (8 * c + 4 * g) * 4); };
  auto ssm_b = [&](float t, int c, int i, int o) AI {
    const float zo = zc[c];
    zc[c] = i == 0 ? rq[c][0] * t : fmaf(acv[0], zo, fmaf(rq[c][i & 3], t, SWP(zo) * acv[1]));
    if (i & 1) ob[o + 4 * c + (i >> 1)] = pk2(zo, zc[c]);
  };
  auto f_vst = [&](int l) AI -> unsigned { return ((l & 31) ^ cg ^ ((l >> 5) << 3)) * 16 + (l >> 5) * 1024; };
  auto f_vag = [&](int l) AI -> unsigned {
    return lds0 + L1_AGG + cg * 8192 + (l & 31) * 64 + ((rg ^ ((l >> 2) & 3)) * 16) + (2 * (l >> 5) + cg) * 4;
  };
  unsigned vst, vag;
  auto ssm_st = [&](int ts, int c) AI {
    const u32x4 o = {ob[4 * c], ob[4 * c + 1], ob[4 * c + 2], ob[4 * c + 3]};
    const char* sb = uptr(ws, (unsigned)OFF_H + (((2 * pi + (rg >> 1)) * 32 + tb0 + ts) * 8 + 4 * (rg & 1) + (c ^ cg)) * 512);
    const unsigned vo = (GT ? f_vst(olane()) : vst) ^ (c * 16);
    GST16S(sb, vo, o);
  };
  auto agg_put = [&](int p, int c, float z) AI {
    const unsigned a = (GT ? f_vag(olane()) : vag) ^ (c * 4);
    if (p == 4) *LP(float, a + (16384 - cg * 8192)) = z;
    else *LP(float, a + p * 2048) = z;
  };
  auto ssm_q = [&](int p, int q) AI {
    const int c = q & 1, i = q >> 1;
    ssm_b(acc[p][8 * c + i], c, i, 0);
    if (i == 3) ssm_mr(c, 1);
    if (i == 7) {
      agg_put(p, c, zc[c]);
      if (p < 3) ssm_st(4 * cg + p, c);
    }
  };
  const unsigned exs = lds0 + L1_AGG;
  auto ss_send = [&](int g) AI {
    *LP(f32x4, exs + w * 2048 + g * 1024 + olane() * 16) = (f32x4){accs[8 + 4 * g], accs[9 + 4 * g], accs[10 + 4 * g], accs[11 + 4 * g]};
  };
  auto ss_recv = [&]() AI {
    const unsigned a = exs + (w ^ 4) * 2048 + olane() * 16;
    pv[0] = *LP(f32x4, a);
    pv[1] = *LP(f32x4, a + 1024);
  };
  auto ss_q = [&](int i) AI {
    ssm_b(accs[i] + pv[i >> 2][i & 3], 0, i, 0);
    if (i == 3) ssm_mr(0, 1);
  };
  f32x4 pz[2];
  f32x2 pa, pb;
  float pcv;
  unsigned pla, pso, pto;
  auto pfx_pre = [&](int ts, int k) AI {
    const int l = olane();
    const unsigned cp = (tb0 + ts) * 32 + (l & 31);
    if (k == 0) {
      pa = GT ? aco : *LP(f32x2, lds0 + L1_AC + (ts * 32 + (l & 31)) * 8);
      pla = (lds0 + L1_AGG + ts * 2048 + (l & 31) * 64 + ((l >> 2) & 3) * 16) ^ (l & 32);
      asm volatile("" : "+v"(pla));
    } else if (k == 1) {
      pa = csq(csq(csq(pa)));
      asm volatile("" : "+v"(pa));
    } else if (k == 2) {
      pb = csq(csq(csq(pa)));
      asm volatile("" : "+v"(pb));
    } else {
      pso = (unsigned)OFF_CAR + pi * 65536 + (l >> 5) * 32768 + cp * 16;
      pto = LLO ? (unsigned)OFF_T0 + (pi * 1024 + cp) * 4 : (unsigned)OFF_TOT + (cp * 16 + pi) * 4;
      asm volatile("" : "+v"(pso), "+v"(pto));
    }
  };
  auto pfx_run = [&]() AI {
    pz[0] = *LP(f32x4, pla);
    pz[1] = *LP(f32x4, pla ^ 16);
#pragma unroll
    for (int s = 0; s < 8; ++s) {
      const float z = pz[s >> 2][s & 3];
      pz[s >> 2][s & 3] = s == 0 ? 0.f : pcv;
      pcv = s == 0 ? z : fmaf(pa[0], pcv, z) + SWP(pcv) * pa[1];
    }
    *(f32x4*)(ws + pso) = (f32x4){pz[0][0], pz[1][0], pz[0][1], pz[1][1]};
    *(f32x4*)(ws + 16384 + pso) = (f32x4){pz[0][2], pz[1][2], pz[0][3], pz[1][3]};
    float t0 = pcv, t1 = pcv;
    asm volatile("s_nop 1\n\tv_permlane32_swap_b32 %0, %1" : "+v"(t0), "+v"(t1));
    const float tot = fmaf(pb[0], t0, pcv) + SWP(t0) * pb[1];
    *(float*)(ws + pto) = LLO ? pcv : tot;
  };
  auto quantum = [&](auto uc) AI {
    constexpr int u = decltype(uc)::value;
    if constexpr (u >= 1 && u < 9) stat_q(u - 1);
    if constexpr (u == 9 || u == 10) ss_send(u - 9);
    if constexpr (u == 13) { stat_q(8); cs_get(4); }
    if constexpr (u >= 15 && u < 19) stat_q(u - 6);
    if constexpr (u == 29 || u == 61 || u == 93 || u == 120) cs_get((u + 8) / 32 - 1);
    if constexpr (u == 14 || u == 30 || u == 62 || u == 94 || u == 122) cs_split(0);
    if constexpr (u == 17 || u == 31 || u == 63 || u == 95 || u == 123) cs_split(1);
    if constexpr (u == 17) corr(accs, true);
    if constexpr (u == 31 || u == 63 || u == 95 || u == 123) corr(acc[(u + 5) / 32 - 1], !GT);
    if constexpr (u == 18) { ssm_pre(8); ss_recv(); ssm_mr(0, 0); }
    if constexpr (u >= 20 && u < 28) ss_q(u - 20);
    if constexpr (u == 28) { agg_put(4, 0, zc[0]); ssm_st(8, 0); }
    if constexpr (u == 19 || u == 32) {
      if (w == PFW) {
        if (u == 19) { pfx_pre(8, 0); pfx_pre(8, 3); pfx_pre(8, 1); pfx_pre(8, 2); }
        else pfx_run();
      }
    }
    if constexpr (GT) {
      if constexpr (u >= 33 && u < 65) gate_q(0, u - 33);
      if constexpr (u >= 65 && u < 73) up_q(1, u - 65);
      if constexpr (u == 69) up_st(1, 0);
      if constexpr (u == 73) up_st(1, 1);
      if constexpr (u >= 97) gate_q(2, u - 97);
    } else {
      sfor<0, 16>([&](auto gc) AI {
        constexpr int i = decltype(gc)::value;
        if constexpr (u == k1_s_unit(0, i)) ssm_q(0, i);
        if constexpr (u == k1_s_unit(1, i)) ssm_q(1, i);
        if constexpr (u == k1_s_unit(2, i)) ssm_q(2, i);
      });
      if constexpr (u == 31 || u == 63 || u == 95 || u == 124) { ssm_pre(4 * cg + (u + 5) / 32 - 1); ssm_mr(0, 0); ssm_mr(1, 0); }
      if constexpr (u == 121 || u >= 125) pfx_pre(w, u == 121 ? 0 : u - 124);
      if constexpr (u == 11) { vst = f_vst(olane()); asm volatile("" : "+v"(vst)); }
      if constexpr (u == 12) { vag = f_vag(olane()); asm volatile("" : "+v"(vag)); }
    }
  };

  bf16x8 f[4];
  auto rd = [&](auto uc) AI {
    constexpr int u = decltype(uc)::value;
    if constexpr (u < 128) {
      constexpr int s = 8 + (u >> 4), k = u & 15, so = (s & 1) * 32768;
      f[u & 3] = wfrag(aw[k >> 2] + so + (k & 3) * 8192);
      if ((s & 1) && k == 15 && s < 15) flip();
    }
  };
  RBAR(8, k1_wait(GT, 8));
  STAMP(4);
  rd(IC<0>{}); rd(IC<1>{}); rd(IC<2>{}); rd(IC<3>{});
  SCHED();
  gr = MFMA32(xf[30], xf[30], gr);
  SCHED();
  issue(IC<9>{}, 2);
  SCHED();
  accs = MFMA32(xf[30], wd2, accs);
  sm = MFMA16(sel, xf[30], sm);
  SCHED();
  issue(IC<9>{}, 3);
  f32x4 csb = zero4;
  SCHED();
  sfor<0, 128>([&](auto uc) AI {
    constexpr int u = decltype(uc)::value;
    constexpr int s = 8 + (u >> 4), k = u & 15;
    constexpr int p = (s - 8) >> 1, half = s & 1;
    constexpr bool first = k == 0 && half == 0;
    if (k < 4) csb = MFMA16(sel, f[u & 3], first ? zero4 : csb);
    acc[p] = mm(f[u & 3], xf[16 * half + 4 * (k & 3) + (k >> 2)], first ? zero16 : acc[p]);
    SCHED();
    if constexpr (half == 1 && k == 6) { csa = csb; cs_put(p); }
    if constexpr (k == 13 && s < 15) {
      RBAR(s + 1, k1_wait(GT, s + 1));
      STAMP(s - 3);
      rd(IC<u + 3>{}); rd(IC<u + 4>{});
    } else if constexpr (k != 12 || s == 15) {
      rd(IC<u + 4>{});
    }
    if constexpr (u == 120) { BARRIER(); }
    sfor<10, 16>([&](auto tc) AI {
#pragma unroll
      for (int d = 0; d < 4; ++d)
        if (u == k1_dma_unit(decltype(tc)::value, d)) issue(tc, d);
    });
#pragma unroll
    for (int i = 0; i < K1_NPI; ++i)
      if (GT && u == k1_pf_unit(i)) prefetch(i);
    if constexpr (GT && u >= PT0 && u < PT0 + 3) ptouch(u - PT0);
    quantum(uc);
    SCHED();
  });
  STAMP(13);
  if (GT) {
    gate_q(2, 31);
#pragma unroll
    for (int k = 0; k < 8; ++k) {
      up_q(3, k);
      if (k == 3) up_st(3, 0);
    }
    up_st(3, 1);
  } else {
#pragma unroll
    for (int q = 0; q < 16; ++q) ssm_q(3, q);
    ssm_st(4 * cg + 3, 0);
    ssm_st(4 * cg + 3, 1);
    BARRIER();
    pfx_run();
  }
  STAMP(14);
  VMCNT(0);
  RT1(0, (pi * 16 + nb) * 8 + w, ws);
}

__global__ __launch_bounds__(512, 2) NOPK void k1(K1Args A) {
  extern __shared__ __attribute__((aligned(16))) char smem[];
  const int b = blockIdx.x;
  const int xcd = b & 7, r = b >> 3;
  const int pl = r & 7, nl = r >> 3;
  const int q = (xcd >> 1) & 1, kk = xcd >> 2;
  const int pi = (xcd & 1) * 8 + pl, nb = nl < 3 ? 7 * kk + 4 * q + nl : ((pl >> 2) == q ? 7 * kk + 3 : 14 + kk);
  const int c0 = (pl + 2 * nl) & 7;
  unsigned long long cpc;
  unsigned clen;
  asm volatile("s_getpc_b64 %0\nk1_pc_ref:\n\ts_mov_b32 %1, k2_code_end-k1_pc_ref" : "=s"(cpc), "=s"(clen));
  clen = clen < 65536u ? clen : 0u;
  if (nb < 14) k1_body<true>(A, smem, pi, nb, c0, r, cpc, clen);
  else k1_body<false>(A, smem, pi, nb, c0, r, cpc, clen);
}

struct K2Args {
  const u16 *x, *Dk, *wd, *Cre, *Cim;
  char* ws;
  float* out;
};
constexpr int L2_EXS = 131072;
constexpr int L2_EXY = 65536;
constexpr int L2_X = L2_EXS + 16384;
constexpr int L2_DUM = L2_X + 10240;
constexpr int L2_SIZE = L2_DUM + 256;
#define QP0 56
#define QPD 2
#define QPE 8
constexpr int k2_pf(int u) {
  for (int j = 0; j < 4; ++j)
    if (u == QP0 + (j >> 1) * QPE + (j & 1) * QPD) return j;
  return -1;
}
#define AY 0
#define XT 6
#define YSC " sc1"
template <int S> __device__ __forceinline__ void dmac(const char* sbase, unsigned voff, unsigned lds_dst) {
  asm volatile("s_mov_b32 m0, %2\n\ts_nop 0\n\tglobal_load_lds_dwordx4 %0, %1" YSC " ; RING %3" ::"v"(voff), "s"(sbase), "s"(lds_dst), "i"(S) : "memory");
}
#define SBAR() do { asm volatile("" ::: "memory"); __builtin_amdgcn_s_barrier(); asm volatile("" ::: "memory"); } while (0)
#define K2ST(k, i)
#define K2OUT(k, w)
#define K2DECL
__device__ __forceinline__ void k2_load(const K2Args& A, unsigned lds0, int w, int ml, int mi, int nj) {
  const int lane = threadIdx.x & 63;
  K2DECL;
  K2ST(3, 0);
  char* ws = A.ws;
  const int r8 = lane >> 3, pc = lane & 7;
  const int rotg = (7 * ml) >> 1;
  const unsigned va = w * 2048 + lane * 16;
  const unsigned vw = (16 * w + r8) * 1024 + 16 * (pc ^ (((r8 >> 1) & 1) << 2));
  const unsigned vc = (8 * w + r8) * 1024 + 16 * (pc ^ ((4 * (w & 1) + (r8 >> 1)) & 7));
  auto issue = [&](auto sc) AI {
    constexpr int s = decltype(sc)::value;
    const unsigned sl = lds0 + (s & 3) * 32768;
    if constexpr (s >= 22) {
    } else if constexpr (s < 14) {
      const int gs = s + rotg >= 14 ? s + rotg - 14 : s + rotg;
      const char* ab = ws + OFF_A + (size_t)(mi * 14 + gs) * 16384;
      const char* wb = (const char*)A.wd + (size_t)gs * 131072 + nj * 128;
      dmar<s>(ab, va, sl + w * 2048);
      dmar<s>(ab + 1024, va, sl + w * 2048 + 1024);
      dmar<s>(wb, vw, sl + 16384 + w * 2048);
      dmar<s>(wb + 8192, vw, sl + 16384 + w * 2048 + 1024);
    } else {
      const int ys = (s - 14 + nj) & 7;
      const char* hb = ws + OFF_HF + (size_t)(mi * 8 + ys) * 16384;
      dmac<s>(hb, va, sl + w * 2048);
      dmac<s>(hb + 1024, va, sl + w * 2048 + 1024);
      dmar<s>((const char*)A.Cre + nj * 65536 + ys * 128, vc, sl + 16384 + w * 1024);
      dmar<s>((const char*)A.Cim + nj * 65536 + ys * 128, vc, sl + 24576 + w * 1024);
    }
  };
  {
    const int s0 = w >> 2;
    const int gs = s0 + rotg;
    const unsigned va0 = lane * 16, vw0 = r8 * 1024 + 16 * (pc ^ (((r8 >> 1) & 1) << 2));
    const char* ab = ws + OFF_A + (size_t)(mi * 14 + gs) * 16384 + (w & 3) * 4096;
    const char* wb = (const char*)A.wd + (size_t)gs * 131072 + nj * 128 + (w & 3) * 32768;
    const unsigned sl = lds0 + s0 * 32768 + (w & 3) * 4096;
#pragma unroll
    for (int d = 0; d < 4; ++d) {
      dma(ab + d * 1024, va0, sl + d * 1024);
      dma(wb + d * 8192, vw0, sl + 16384 + d * 1024);
    }
  }
  K2ST(3, 2);
  issue(IC<2>{});
  K2ST(3, 3);
  if (w < 4) VMCNT(4);
  SBAR();
  K2ST(3, 4);
  sfor<0, 21>([&](auto tc) AI {
    constexpr int t = decltype(tc)::value;
    if constexpr (t == XT) {
      dma((const char*)A.x + (size_t)mi * 65536 + nj * 128, (8 * w + r8) * 1024 + pc * 16, lds0 + L2_X + w * 1024);
      if (w == 0) dma(ws + OFF_ST + mi * 512, (lane & 31) * 16, lds0 + L2_X + 8192);
      if (w == 1) dma((const char*)A.Dk + nj * 128, pc * 16, lds0 + L2_X + 9216);
    }
    issue(IC<t + 3>{});
    RBAR(t + 1, (t + 2 < 22 ? 4 : 0) + (t + 3 < 22 ? 4 : 0));
    if (t & 1) K2ST(3, 5 + (t >> 1));
    if (t == 20) K2ST(3, 15);
  });
  K2OUT(3, w);
}

__device__ __forceinline__ float mulc(f32x2 c, float v) { return fmaf(c[0], v, SWP(v) * c[1]); }
__device__ __forceinline__ float fmac(f32x2 c, float v, float t) { return fmaf(SWP(v), c[1], fmaf(c[0], v, t)); }
__device__ __forceinline__ float bfadd(unsigned pair, unsigned selv, float c) {
  return __builtin_amdgcn_fdot2_f32_bf16(__builtin_bit_cast(bf16x2_t, pair), __builtin_bit_cast(bf16x2_t, selv), c, false);
}

struct K2Out {
  float sgv[8], xfv[8], rs;
  u32x2 xq[2], dq;
  f32x2 st[2];
  __device__ __forceinline__ void load(unsigned lds0, int w, int eh, int lane) {
    const int r = 32 * (w & 1) + 16 * eh + (lane >> 3);
    const unsigned co = (w >> 1) * 64 + (lane & 7) * 8;
#pragma unroll
    for (int e = 0; e < 2; ++e) {
      xq[e] = *LP(u32x2, lds0 + L2_X + (r + 8 * e) * 128 + co);
      st[e] = *LP(f32x2, lds0 + L2_X + 8192 + (r + 8 * e) * 8);
    }
    dq = *LP(u32x2, lds0 + L2_X + 9216 + co);
  }
  __device__ __forceinline__ void prep(int v) {
    const int e = v >> 2;
    if (!(v & 3)) rs = __builtin_amdgcn_rcpf(st[e][1]);
    const unsigned xw = xq[e][(v >> 1) & 1], dw = dq[(v >> 1) & 1];
    const float xx = bf2f((xw >> (16 * (v & 1))) & 0xffff), dd = bf2f((dw >> (16 * (v & 1))) & 0xffff);
    const float xn = rbf(rbf(xx - st[e][0]) * rs);
    xfv[v] = xx;
    sgv[v] = rbf(xn * dd);
    asm volatile("" : "+v"(xfv[v]), "+v"(sgv[v]));
  }
  __device__ __forceinline__ void finish(float* out, unsigned lds0, int w, int eh, int lane, int mi, int nj) {
    const int r8 = lane >> 3, pc = lane & 7;
    const unsigned exr = lds0 + w * 4096 + eh * 2048 + r8 * 128 + ((pc ^ r8) * 16);
    const unsigned eo = (64 * mi + 32 * (w & 1) + 16 * eh + r8) * D_ + 64 * nj + 32 * (w >> 1) + 4 * pc;
#pragma unroll
    for (int e = 0; e < 2; ++e) {
      const f32x4 y = *LP(f32x4, exr + L2_EXY + e * 1024), sv = *LP(f32x4, exr + L2_EXS + e * 1024);
      f32x4 o;
#pragma unroll
      for (int rr = 0; rr < 4; ++rr) o[rr] = fmaf(fmaf(2.f, y[rr], sgv[4 * e + rr]), sv[rr], xfv[4 * e + rr]);
      asm volatile("global_store_dwordx4 %0, %1, %2 sc0 sc1\n\ts_nop 1" ::"v"((eo + 8 * e * D_) * 4), "v"(o), "s"(out) : "memory");
    }
  }
};

__device__ __forceinline__ void k2_gate(const K2Args& A, unsigned lds0, int w, int mi, int nj) {
  const int lane = threadIdx.x & 63;
  char* ws = A.ws;
  K2DECL;
  K2ST(2, 0);
  RT0();
  const int wr = w & 1, wc = w >> 1;
  const int n = lane & 31, h = lane >> 5, t16 = lane & 15, g1 = (lane >> 4) & 1;
  const unsigned exw = lds0 + w * 4096 + n * 128 + ((h ^ (n & 7)) * 16);
  const unsigned arb = lds0 + wr * 1024 + lane * 16;
  const unsigned trw = lds0 + 16384 + (4 * h + (t16 >> 2)) * 128 + 32 * ((2 * wc + g1) ^ ((t16 >> 3) << 1)) + 8 * (t16 & 3);
  f32x16 acc;
#pragma unroll
  for (int i = 0; i < 16; ++i) acc[i] = 0.f;
  bf16x8 fa[4], fb[4];
  K2Out O;
  unsigned ga[2] = {arb, arb + 65536}, gb[2] = {trw, trw + 65536};
  asm volatile("" : "+v"(ga[1]), "+v"(gb[1]));
  auto rd = [&](auto uc) AI {
    constexpr int u = decltype(uc)::value, s = u >> 3, j = u & 7, so = (s & 1) * 32768, hf = (s >> 1) & 1;
    if constexpr (u < 112) {
      fa[u & 3] = *LP(bf16x8, ga[hf] + so + j * 2048);
      fb[u & 3] = trfrag(gb[hf] + so + j * 2048, 1024);
    }
  };
  const char* pfb = (const char*)(w < 2 ? A.Cre : A.Cim) + nj * 65536 + (mi & 3) * 16384 + (w & 1) * 8192;
  SBAR();
  K2ST(2, 1);
  rd(IC<0>{}); rd(IC<1>{}); rd(IC<2>{});
  SCHED();
  sfor<0, 112>([&](auto uc) AI {
    constexpr int u = decltype(uc)::value;
    acc = MFMA32(fb[u & 3], fa[u & 3], acc);
    SCHED();
    if constexpr ((u & 7) == 5) BARRIER();
    rd(IC<u + 3>{});
    if constexpr (k2_pf(u) >= 0)
      asm volatile("s_mov_b32 m0, %2\n\ts_cmp_eq_u32 %3, %4\n\ts_cselect_b64 exec, -1, 0\n\tglobal_load_lds_dword %0, %1\n\ts_mov_b64 exec, -1"
                   ::"v"(lane * 128), "s"(pfb), "s"(lds0 + L2_DUM), "s"(w), "i"(k2_pf(u)) : "memory", "scc");
    if constexpr (u == 74) O.load(lds0, w, 0, lane);
    if constexpr (u >= 78 && u < 110 && (u & 3) == 2) O.prep((u - 78) >> 2);
    SCHED();
    if ((u & 15) == 15) K2ST(2, 2 + (u >> 4));
  });
  VMCNT(0);
#pragma unroll
  for (int q = 0; q < 6; ++q) {
#pragma unroll
    for (int v = (16 * q) / 6; v < (16 * q + 16) / 6; ++v) {
      const float g = rbf(acc[v]);
      acc[v] = rbf(g * __builtin_amdgcn_rcpf(1.f + __expf(-g)));
    }
    asm volatile("" : "+v"(acc));
    SBAR();
  }
  SBAR();
#pragma unroll
  for (int g = 0; g < 4; ++g) {
    const f32x4 o = {acc[4 * g], acc[4 * g + 1], acc[4 * g + 2], acc[4 * g + 3]};
    const unsigned a = exw ^ (g << 5);
    asm volatile("ds_write_b128 %0, %1" ::"v"(a + L2_EXS), "v"(o) : "memory");
  }
  K2ST(2, 9);
  BARRIER();
  K2ST(2, 10);
  O.finish(A.out, lds0, w, 0, lane, mi, nj);
  K2ST(2, 11);
  RT1(1, blockIdx.x * 8 + w, ws);
  RT1(1, blockIdx.x * 8 + w + 4, ws);
}

__device__ __forceinline__ void k2_state(const K2Args& A, unsigned lds0, int w, int mi, int nj) {
  const int lane = threadIdx.x & 63, pi = mi >> 1;
  char* ws = A.ws;
  const int sg = lane >> 4;
  const unsigned cp = nj * 128 + 32 * w + (lane & 15);
  const unsigned hoff = w * 4096 + sg * 512 + (((lane & 15) ^ (sg & 1) ^ ((sg >> 1) << 3)) * 16);
  f32x2 a1[2], cr[2], fc[2];
  f32x4 tq[2], a4[2];
  float th[2];
  u32x4 hq[2][2];
  {
    const unsigned o0 = cp * 16, o1 = cp * 64 + (12 - 4 * sg) * 4, o2 = cp * 16 + (sg >> 1) * 16384 + (sg & 1) * 8;
    const char* b0 = ws + OFF_AT;
    const char* b0b = b0 + 256;
    GLD16S(a4[0], o0, b0); GLD16S(a4[1], o0, b0b);
#pragma unroll
    for (int j = 0; j < 2; ++j) {
      const char* b1 = ws + OFF_TOT + (pi - 16) * 4 + j * 1024;
      const char* b2 = ws + OFF_CAR + pi * 65536 + (mi & 1) * 32768 + j * 256;
      const char* b4 = ws + OFF_T0 + pi * 4096 + j * 64;
      GLD16S(tq[j], o1, b1); GLD8S(cr[j], o2, b2);
      asm volatile("global_load_dword %0, %1, %2" : "=v"(th[j]) : "v"(o0 >> 2), "s"(b4) : "memory");
    }
  }
  auto hload = [&](int j, int c) AI {
    const char* b3 = ws + OFF_H + (size_t)(mi * 8 + nj) * 16384 + j * 256 + c * 2048;
    GLD16S(hq[j][c], hoff, b3);
  };
  asm volatile("s_waitcnt vmcnt(6)" : "+v"(a4[0]), "+v"(a4[1])::"memory");
#pragma unroll
  for (int j = 0; j < 2; ++j) { a1[j] = (f32x2){a4[j][0], a4[j][1]}; fc[j] = (f32x2){a4[j][2], a4[j][3]}; }
  f32x2 a8[2], a16[2], a32[2], a64[2], aP[2], fF[2], fG[2];
#pragma unroll
  for (int j = 0; j < 2; ++j) {
    a8[j] = csq(csq(csq(a1[j]))); a16[j] = csq(a8[j]); a32[j] = csq(a16[j]); a64[j] = csq(a32[j]); aP[j] = csq(a64[j]);
  }
  asm volatile("" : "+v"(aP[0]), "+v"(aP[1]));
  SBAR();
  hload(0, 0); hload(0, 1);
  const f32x2 one = {1.f, 0.f};
#pragma unroll
  for (int j = 0; j < 2; ++j) {
    const f32x2 p4 = csq(csq(aP[j])), p8 = csq(p4);
    fF[j] = cmul(LB4 ? p4 : one, LLO ? one : p8);
    fG[j] = cmul(a1[j], cmul(LB4 ? a8[j] : one, LLO ? one : a16[j]));
  }
  asm volatile("" : "+v"(fF[0]), "+v"(fF[1]), "+v"(fG[0]), "+v"(fG[1]));
  SBAR();
  hload(1, 0); hload(1, 1);
  asm volatile("s_waitcnt vmcnt(4)" : "+v"(cr[0]), "+v"(cr[1]), "+v"(tq[0]), "+v"(tq[1]), "+v"(th[0]), "+v"(th[1])::"memory");
  unsigned slo = 0x00003F80u, shi = 0x3F800000u;
  asm volatile("" : "+s"(slo), "+s"(shi));
  float pr[2][2];
  {
    const int i0 = pi - 4 - 4 * sg;
    float hs[2], b[2];
#pragma unroll
    for (int j = 0; j < 2; ++j) {
      hs[j] = i0 >= 0 ? tq[j][0] : 0.f;
#pragma unroll
      for (int m = 1; m < 4; ++m) hs[j] = fmac(aP[j], hs[j], i0 + m >= 0 ? tq[j][m] : 0.f);
      hs[j] = mulc(fF[j], hs[j]);
      b[j] = hs[j];
    }
    asm volatile("s_nop 1\n\tv_permlane16_swap_b32 %0, %1\n\tv_permlane16_swap_b32 %2, %3" : "+v"(hs[0]), "+v"(b[0]), "+v"(hs[1]), "+v"(b[1]));
    hs[0] += b[0]; hs[1] += b[1];
    b[0] = hs[0]; b[1] = hs[1];
    asm volatile("s_nop 1\n\tv_permlane32_swap_b32 %0, %1\n\tv_permlane32_swap_b32 %2, %3" : "+v"(hs[0]), "+v"(b[0]), "+v"(hs[1]), "+v"(b[1]));
#pragma unroll
    for (int j = 0; j < 2; ++j) {
      const float se = hs[j] + b[j], g0 = mulc(fG[j], (mi & 1) ? fmac(a64[j], se, th[j]) : se), g1v = mulc(a32[j], g0);
      pr[j][0] = fmac(a1[j], cr[j][0], g0);
      pr[j][1] = fmac(a1[j], cr[j][1], g1v);
    }
    asm volatile("" : "+v"(pr[0][0]), "+v"(pr[0][1]), "+v"(pr[1][0]), "+v"(pr[1][1]));
  }
  SBAR();
#pragma unroll
  for (int j = 0; j < 2; ++j) {
    asm volatile("s_waitcnt vmcnt(2)" : "+v"(hq[j][0]), "+v"(hq[j][1])::"memory");
#pragma unroll
    for (int q = 0; q < 4; ++q) {
#pragma unroll
      for (int c = 0; c < 2; ++c) {
        const float p1 = mulc(a1[j], pr[j][c]);
        hq[j][c][q] = pk2(mulc(fc[j], bfadd(hq[j][c][q], slo, pr[j][c])), mulc(fc[j], bfadd(hq[j][c][q], shi, p1)));
        if (q < 3) pr[j][c] = mulc(a1[j], p1);
      }
    }
#pragma unroll
    for (int c = 0; c < 2; ++c) {
      const char* sb = ws + OFF_HF + (size_t)(mi * 8 + nj) * 16384 + j * 256 + c * 2048;
      GST16S(sb, hoff, hq[j][c]);
    }
    SBAR();
  }
  SBAR();
  SBAR();
  VMCNT(0);
  SBAR();
  const char* cb = ws + OFF_CNT + mi * 128;
  const unsigned vz = 0;
  unsigned cnt;
  if (w == 0 && lane == 0) asm volatile("global_atomic_add %0, %1, %2 sc1" ::"v"(vz), "v"(1u), "s"(cb) : "memory");
  SBAR();
  SBAR();
  if (w == 0) asm volatile("global_load_dword %0, %1, %2 sc1" : "=v"(cnt) : "v"(vz), "s"(cb) : "memory");
  const int wr = w & 1, wc = w >> 1;
  const int n = lane & 31, h = lane >> 5, t16 = lane & 15, g1 = (lane >> 4) & 1;
  const unsigned exw = lds0 + w * 4096 + n * 128 + ((h ^ (n & 7)) * 16);
  const unsigned trh = lds0 + (4 * wr + 2 * g1 + ((t16 >> 1) & 1)) * 512 + ((16 * h + 8 * g1 + 2 * (t16 >> 2) + ((t16 >> 1) & 1)) * 16) + (t16 & 1) * 8;
  const int cd = 32 * wc + n;
  const unsigned crb = lds0 + 16384 + cd * 128 + 16 * (h ^ ((cd >> 1) & 7));
  unsigned ya[2][4], yc[2][4];
#pragma unroll
  for (int i = 0; i < 4; ++i) {
    ya[0][i] = trh ^ ((i & 1) * 16) ^ ((i >> 1) * 128); ya[1][i] = ya[0][i] + 65536;
    yc[0][i] = crb ^ (i << 5); yc[1][i] = yc[0][i] + 65536;
  }
  asm volatile("" : "+v"(ya[1][0]), "+v"(ya[1][1]), "+v"(ya[1][2]), "+v"(ya[1][3]));
  asm volatile("" : "+v"(yc[1][0]), "+v"(yc[1][1]), "+v"(yc[1][2]), "+v"(yc[1][3]));
  SBAR();
  if (w == 0) {
    asm volatile("s_waitcnt vmcnt(0)" : "+v"(cnt)::"memory");
    while (__builtin_amdgcn_readfirstlane(cnt) < 8u) {
      __builtin_amdgcn_s_sleep(2);
      asm volatile("global_load_dword %0, %1, %2 sc1\n\ts_waitcnt vmcnt(0)" : "=v"(cnt) : "v"(vz), "s"(cb) : "memory");
    }
  }
  SBAR();
  K2Out O;
  O.load(lds0, w, 1, lane);
#pragma unroll
  for (int v = 0; v < 3; ++v) O.prep(v);
  SBAR();
#pragma unroll
  for (int v = 3; v < 6; ++v) O.prep(v);
  SBAR();
#pragma unroll
  for (int v = 6; v < 8; ++v) O.prep(v);
  f32x16 acc;
#pragma unroll
  for (int i = 0; i < 16; ++i) acc[i] = 0.f;
  bf16x8 fa[4], fb[4];
  auto rd = [&](auto uc) AI {
    constexpr int u = decltype(uc)::value, s = 14 + (u >> 3), i = (u >> 1) & 3, part = u & 1, so = (s & 1) * 32768, hf = (s >> 1) & 1;
    if constexpr (u < 64) {
      fa[u & 3] = trfrag2(ya[hf][part] + so + i * 4096, ya[hf][2 + part] + so + i * 4096);
      fb[u & 3] = *LP(bf16x8, yc[hf][i] + so + part * 8192);
    }
  };
  __builtin_amdgcn_s_setprio(2);
  SBAR();
  rd(IC<0>{}); rd(IC<1>{}); rd(IC<2>{});
  SCHED();
  sfor<0, 64>([&](auto uc) AI {
    constexpr int u = decltype(uc)::value;
    acc = MFMA32(fb[u & 3], fa[u & 3], acc);
    SCHED();
    if constexpr ((u & 7) == 5 && u < 56) BARRIER();
    rd(IC<u + 3>{});
    SCHED();
  });
#pragma unroll
  for (int g = 0; g < 4; ++g)
    *LP(f32x4, (exw ^ (g << 5)) + L2_EXY) = (f32x4){acc[4 * g], acc[4 * g + 1], acc[4 * g + 2], acc[4 * g + 3]};
  BARRIER();
  O.finish(A.out, lds0, w, 1, lane, mi, nj);
}

__global__ __launch_bounds__(1024, 4) NOPK void k2(K2Args A) {
  extern __shared__ __attribute__((aligned(16))) char smem[];
  const unsigned lds0 = (unsigned)(uintptr_t)(LAS char*)smem;
  const int w = __builtin_amdgcn_readfirstlane(threadIdx.x >> 6) & 15;
  const int b = blockIdx.x;
  const int xcd = b & 7, r = b >> 3;
  const int ml = r >> 3, mi = xcd * 4 + ml, nj = r & 7;
  if (w < 4) {
    k2_state(A, lds0, w, mi, nj);
  } else if (w < 12) {
    k2_load(A, lds0, w - 4, ml, mi, nj);
  } else {
    __builtin_amdgcn_s_setprio(1);
    k2_gate(A, lds0, w & 3, mi, nj);
  }
  asm volatile("k2_code_end:");
}

extern "C" void kernel_launch(void* const* d_in, const int* in_sizes, int n_in, void* d_out, int out_size, void* d_ws,
                              size_t ws_size, hipStream_t stream) {
  K1Args a;
  a.x = (const u16*)d_in[0]; a.Lre = (const u16*)d_in[1]; a.Lim = (const u16*)d_in[2];
  a.Bre = (const u16*)d_in[3]; a.Bim = (const u16*)d_in[4]; a.Cre = (const u16*)d_in[5]; a.Cim = (const u16*)d_in[6];
  a.ldt = (const u16*)d_in[7]; a.wg = (const u16*)d_in[10]; a.wu = (const u16*)d_in[11]; a.wd = (const u16*)d_in[12];
  a.ws = (char*)d_ws;
  (void)hipFuncSetAttribute((const void*)k1, hipFuncAttributeMaxDynamicSharedMemorySize, L1_SIZE);
  (void)hipFuncSetAttribute((const void*)k2, hipFuncAttributeMaxDynamicSharedMemorySize, L2_SIZE);
  k1<<<256, 512, L1_SIZE, stream>>>(a);
  K2Args b;
  b.x = (const u16*)d_in[0]; b.Dk = (const u16*)d_in[8]; b.wd = (const u16*)d_in[12];
  b.Cre = (const u16*)d_in[5]; b.Cim = (const u16*)d_in[6];
  b.ws = (char*)d_ws; b.out = (float*)d_out;
  k2<<<256, 1024, L2_SIZE, stream>>>(b);
}
```
